```python
import torch

def _softmax(x, axis=-1):
    return torch.softmax(x, dim=axis)
def _gelu(x):
    return torch.nn.functional.gelu(x, approximate="tanh")
def _silu(x):
    return torch.nn.functional.silu(x)
def _sigmoid(x):
    return torch.sigmoid(x)
def _layernorm(x, axis=-1, eps=1e-5):
    m = x.mean(dim=axis, keepdim=True)
    v = x.var(dim=axis, keepdim=True, unbiased=False)
    return (x - m) / torch.sqrt(v + eps)
def _scatter_add(tgt, idx, src, axis=0):
    return tgt.clone().index_add_(axis, idx, src)
def _segment_sum(src, seg, n):
    out = torch.zeros((n,) + src.shape[1:], dtype=src.dtype)
    return out.index_add_(0, seg, src)
def _pad(x, pw):
    flat = tuple(v for pair in reversed(pw) for v in pair)
    return torch.nn.functional.pad(x, flat)

kernel_name = 'concat_heads__mha_block_cached__gqa_block_cached__near_ridge'

def setup_inputs():

    _w = (lambda a: (lambda t: t.to(torch.bfloat16) if t.is_floating_point() else t)(torch.as_tensor(a)))
    def _A_setup():
        B, Sq, D = 8, 144, 512
        H, Sk, Dh = 32, 512, 128
        return {
            "x": _w((lambda *s: torch.randn(*s, dtype=torch.bfloat16) * 0.8)(B, Sq, D)),
            "k_cache": _w((lambda *s: torch.randn(*s, dtype=torch.bfloat16) * 0.8)(B, H, Sk, Dh)),
            "v_cache": _w((lambda *s: torch.randn(*s, dtype=torch.bfloat16) * 0.05)(B, H, Sk, Dh)),
            "wq": _w((lambda *s: torch.randn(*s, dtype=torch.bfloat16) * 0.8)(D, H * Dh) * (D ** -0.5)),
            "wo": _w((lambda *s: torch.randn(*s, dtype=torch.bfloat16) * 0.05)(H * Dh, D) * ((H * Dh) ** -0.5)),
        }
    _A_in = _A_setup()

    def _B_setup():
        B, Sq, D = 8, 144, 512
        Hq, Hkv, Sk, Dh = 32, 32, 512, 128
        assert Hq % Hkv == 0, (Hq, Hkv)
        return {
            "x": _w((lambda *s: torch.randn(*s, dtype=torch.bfloat16) * 0.8)(B, Sq, D)),
            "k_cache": _w((lambda *s: torch.randn(*s, dtype=torch.bfloat16) * 0.8)(B, Hkv, Sk, Dh)),
            "v_cache": _w((lambda *s: torch.randn(*s, dtype=torch.bfloat16) * 0.05)(B, Hkv, Sk, Dh)),
            "wq": _w((lambda *s: torch.randn(*s, dtype=torch.bfloat16) * 0.8)(D, Hq * Dh) * (D ** -0.5)),
            "wo": _w((lambda *s: torch.randn(*s, dtype=torch.bfloat16) * 0.05)(Hq * Dh, D) * ((Hq * Dh) ** -0.5)),
        }
    _B_in = _B_setup()

    return {
        "A_x": _A_in["x"],
        "A_k_cache": _A_in["k_cache"],
        "A_v_cache": _A_in["v_cache"],
        "A_wq": _A_in["wq"],
        "A_wo": _A_in["wo"],
        "B_x": _B_in["x"],
        "B_k_cache": _B_in["k_cache"],
        "B_v_cache": _B_in["v_cache"],
        "B_wq": _B_in["wq"],
        "B_wo": _B_in["wo"],
    }

def reference(A_x, A_k_cache, A_v_cache, A_wq, A_wo, B_x, B_k_cache, B_v_cache, B_wq, B_wo):
    def _A_ref(x, k_cache, v_cache, wq, wo):
        B, Sq, D = x.shape
        H, Dh = 32, 128
        q = (lambda x, *d: x.permute(*d))((x @ wq).reshape(B, Sq, H, Dh), 0, 2, 1, 3)
        scores = torch.einsum("bhqd,bhkd->bhqk", q, k_cache) * (Dh ** -0.5)
        p = _softmax(scores, axis=-1)
        o = torch.einsum("bhqk,bhkd->bhqd", p, v_cache)
        return (lambda x, *d: x.permute(*d))(o, 0, 2, 1, 3).reshape(B, Sq, H * Dh) @ wo

    def _B_ref(x, k_cache, v_cache, wq, wo):
        B, Sq, D = x.shape
        Hq, Dh = 32, 128
        Hkv = k_cache.shape[1]
        rep = Hq // Hkv
        q = (lambda x, *d: x.permute(*d))((x @ wq).reshape(B, Sq, Hq, Dh), 0, 2, 1, 3)
        k = (lambda x, n, axis: torch.repeat_interleave(x, n, dim=axis))(k_cache, rep, axis=1)
        v = (lambda x, n, axis: torch.repeat_interleave(x, n, dim=axis))(v_cache, rep, axis=1)
        scores = torch.einsum("bhqd,bhkd->bhqk", q, k) * (Dh ** -0.5)
        p = _softmax(scores, axis=-1)
        o = torch.einsum("bhqk,bhkd->bhqd", p, v)
        return (lambda x, *d: x.permute(*d))(o, 0, 2, 1, 3).reshape(B, Sq, Hq * Dh) @ wo

    a = _A_ref(A_x, A_k_cache, A_v_cache, A_wq, A_wo)
    b = _B_ref(B_x, B_k_cache, B_v_cache, B_wq, B_wo)
    return torch.concatenate([a, b], axis=-1)
```

```cpp
#include <hip/hip_runtime.h>
#include <hip/hip_bf16.h>
#include <stdint.h>

typedef short bf16x8 __attribute__((ext_vector_type(8)));
typedef short s16x4 __attribute__((ext_vector_type(4)));
typedef float f32x4 __attribute__((ext_vector_type(4)));
typedef float f32x2 __attribute__((ext_vector_type(2)));
typedef __bf16 bf16x2 __attribute__((ext_vector_type(2)));
typedef unsigned u32x4 __attribute__((ext_vector_type(4)));
typedef __attribute__((address_space(3))) const char* lds_cptr;
typedef __attribute__((address_space(1))) unsigned gu32;

constexpr int NB = 8, SQ = 144, DM = 512, NH = 32, SK = 512, DH = 128, NQT = SQ / 16;
constexpr float QSCALE = 0.08838834764831845f * 1.4426950408889634f;
constexpr float PBIAS = 7.0f;

constexpr size_t WS_CNT = 0;
constexpr size_t WS_O = 65536;
constexpr size_t WS_O_BYTES = (size_t)2 * NB * NH * 4 * NQT * 1024;
constexpr size_t WS_PART = WS_O + WS_O_BYTES;

#define MFMA(a, b, c) __builtin_amdgcn_mfma_f32_16x16x32_bf16(a, b, c, 0, 0, 0)
#define WAIT_BAR(N) asm volatile("s_waitcnt vmcnt(" #N ") lgkmcnt(0)\n\ts_barrier" ::: "memory")

__device__ __forceinline__ unsigned pk_bf16(float a, float b) {
    f32x2 v = {a, b};
    return __builtin_bit_cast(unsigned, __builtin_convertvector(v, bf16x2));
}
__device__ __forceinline__ bf16x8 lds_row_frag(lds_cptr p) { return *(const __attribute__((address_space(3))) bf16x8*)p; }
__device__ __forceinline__ bf16x8 lds_tr_frag(lds_cptr p) {
    s16x4 lo = __builtin_amdgcn_ds_read_tr16_b64_v4i16((__attribute__((address_space(3))) s16x4*)p);
    s16x4 hi = __builtin_amdgcn_ds_read_tr16_b64_v4i16((__attribute__((address_space(3))) s16x4*)(p + 4096));
    return (bf16x8){lo[0], lo[1], lo[2], lo[3], hi[0], hi[1], hi[2], hi[3]};
}
__device__ __forceinline__ void glds1(unsigned voff, const char* sbase, unsigned m0v) {
    asm volatile("s_mov_b32 m0, %1\n\ts_nop 0\n\tglobal_load_lds_dwordx4 %0, %2" ::"v"(voff), "s"(m0v), "s"(sbase) : "memory");
}
__device__ __forceinline__ void glds2(unsigned voff, const char* sbase, unsigned dst) {
    asm volatile("s_mov_b32 m0, %1\n\ts_nop 0\n\tglobal_load_lds_dwordx4 %0, %3\n\t"
                 "s_mov_b32 m0, %2\n\ts_nop 0\n\tglobal_load_lds_dwordx4 %0, %3 offset:128"
                 ::"v"(voff), "s"(dst), "s"(dst + 896), "s"(sbase) : "memory");
}
__device__ __forceinline__ void glds4(unsigned voff, const char* sbase, unsigned dst) {
    asm volatile("s_mov_b32 m0, %1\n\ts_nop 0\n\tglobal_load_lds_dwordx4 %0, %3\n\tglobal_load_lds_dwordx4 %0, %3 offset:2048\n\t"
                 "s_mov_b32 m0, %2\n\ts_nop 0\n\tglobal_load_lds_dwordx4 %0, %3 offset:128\n\tglobal_load_lds_dwordx4 %0, %3 offset:2176"
                 ::"v"(voff), "s"(dst), "s"(dst + 896), "s"(sbase) : "memory");
}
__device__ __forceinline__ u32x4 regroup(unsigned a0, unsigned a1, unsigned b0, unsigned b1) {
    auto r0 = __builtin_amdgcn_permlane32_swap(a0, b0, false, false);
    auto r1 = __builtin_amdgcn_permlane32_swap(a1, b1, false, false);
    auto x0 = __builtin_amdgcn_permlane16_swap(r0[0], r0[1], false, false);
    auto x1 = __builtin_amdgcn_permlane16_swap(r1[0], r1[1], false, false);
    return (u32x4){x0[0], x1[0], x0[1], x1[1]};
}
__device__ __forceinline__ float xor16_max(float v) {
    auto r = __builtin_amdgcn_permlane16_swap(__float_as_uint(v), __float_as_uint(v), false, false);
    return fmaxf(__uint_as_float(r[0]), __uint_as_float(r[1]));
}
__device__ __forceinline__ float xor32_max(float v) {
    auto r = __builtin_amdgcn_permlane32_swap(__float_as_uint(v), __float_as_uint(v), false, false);
    return fmaxf(__uint_as_float(r[0]), __uint_as_float(r[1]));
}
__device__ __forceinline__ float xor16_sum(float v) {
    auto r = __builtin_amdgcn_permlane16_swap(__float_as_uint(v), __float_as_uint(v), false, false);
    return __uint_as_float(r[0]) + __uint_as_float(r[1]);
}
__device__ __forceinline__ float xor32_sum(float v) {
    auto r = __builtin_amdgcn_permlane32_swap(__float_as_uint(v), __float_as_uint(v), false, false);
    return __uint_as_float(r[0]) + __uint_as_float(r[1]);
}

constexpr int KV_SLOT = 32768, KV_NSLOT = 5;
constexpr int W_RING = 65536, W_SLOT = 16384, X_RING = W_RING + 3 * W_SLOT, X_SLOT = 18432;
constexpr int ATTN_LDS = KV_SLOT * KV_NSLOT;

template <int NT>
__device__ __forceinline__ void attn_body(const char* xg, const char* wg, const char* kvg, char* og, int w, int qt0, lds_cptr lds,
                                          unsigned lds0) {
    const int lane = threadIdx.x & 63, r = lane & 15, g = lane >> 4;
    const int hsel = w >> 2, a = w & 3;
    const int e = (lane >> 1) & 7, colb = (lane >> 4) * 32 + (lane & 1) * 16;
    const unsigned voff_kv = e * 256 + colb;
    const unsigned voff_x = e * (DM * 2) + colb;
    const unsigned voff_w = (8 * (2 * (a & 1) + (e >> 2)) + 4 * (a >> 1) + (e & 3)) * (NH * DH * 2) + colb;
    const unsigned kv_dst = lds0 + w * 4096;
    const unsigned w_dst = lds0 + W_RING + hsel * 8192 + a * 2048;
    const unsigned x_dst = lds0 + X_RING + w * 1024;
    const char* xs = xg + (size_t)w * 8 * (DM * 2);
    const int row_off = (r >> 3) * 2048 + (g >> 1) * 256 + (r & 7) * 32 + (g & 1) * 16;
    const int tr_off = (g >> 1) * 2048 + (4 * (g & 1) + (r >> 2)) * 32 + (lane & 3) * 8;
    const int x_off = (2 * qt0 + (r >> 3)) * 1024 + (g >> 1) * 256 + (r & 7) * 32 + (g & 1) * 16;

    auto issue_x = [&](int j) {
        const char* s = xs + j * 128;
        const unsigned d = x_dst + (j & 1) * X_SLOT;
        if (w < 2) glds1(voff_x, s + 128 * (DM * 2), d + 16 * 1024);
        glds1(voff_x, s, d);
        glds1(voff_x, s + 64 * (DM * 2), d + 8 * 1024);
    };
    auto issue_w = [&](int i, int slot) { glds2(voff_w, wg + (size_t)i * 32 * (NH * DH * 2), w_dst + slot * W_SLOT); };
    auto issue_kv = [&](int st, int slot) { glds4(voff_kv, kvg + (size_t)st * 8192, kv_dst + slot * KV_SLOT); };

    issue_x(0); issue_w(0, 0); issue_w(1, 1); issue_x(1); issue_w(2, 2);
    issue_kv(0, 0); issue_kv(1, 1);

    f32x4 acc[NT][8];
#pragma unroll
    for (int t = 0; t < NT; ++t)
#pragma unroll
        for (int d = 0; d < 8; ++d) acc[t][d] = (f32x4){0.f, 0.f, 0.f, 0.f};

    {
        int slot = 0;
        for (int i = 0; i < 16; ++i) {
            if (i == 0) WAIT_BAR(14);
            else if (i == 1) WAIT_BAR(12);
            else if (i == 2) WAIT_BAR(10);
            else if (i == 15) WAIT_BAR(0);
            else if (i & 1) WAIT_BAR(4);
            else WAIT_BAR(2);
            const int prev = slot == 0 ? 2 : slot - 1;
            if (i >= 2 && i <= 12 && !(i & 1)) issue_x((i >> 1) + 1);
            if (i >= 1 && i <= 13) issue_w(i + 2, prev);
            lds_cptr xp = lds + X_RING + ((i >> 1) & 1) * X_SLOT + (i & 1) * 512 + x_off;
            lds_cptr wp = lds + W_RING + slot * W_SLOT + hsel * 8192 + tr_off;
            bf16x8 xf[NT];
#pragma unroll
            for (int t = 0; t < NT; ++t) xf[t] = lds_row_frag(xp + t * 2048);
#pragma unroll
            for (int d = 0; d < 8; ++d) {
                const bf16x8 wf = lds_tr_frag(wp + d * 256);
#pragma unroll
                for (int t = 0; t < NT; ++t) acc[t][d] = MFMA(wf, xf[t], acc[t][d]);
            }
            slot = slot == 2 ? 0 : slot + 1;
        }
    }
    bf16x8 qf[NT][4];
#pragma unroll
    for (int t = 0; t < NT; ++t)
#pragma unroll
        for (int s = 0; s < 4; ++s) {
            const f32x4 u = acc[t][2 * s] * QSCALE, v = acc[t][2 * s + 1] * QSCALE;
            qf[t][s] = __builtin_bit_cast(bf16x8, regroup(pk_bf16(u[0], u[1]), pk_bf16(u[2], u[3]), pk_bf16(v[0], v[1]), pk_bf16(v[2], v[3])));
        }
#pragma unroll
    for (int t = 0; t < NT; ++t)
#pragma unroll
        for (int d = 0; d < 8; ++d) acc[t][d] = (f32x4){0.f, 0.f, 0.f, 0.f};

    f32x4 negm[NT];
    float lsum[NT];
#pragma unroll
    for (int t = 0; t < NT; ++t) { negm[t] = (f32x4){0.f, 0.f, 0.f, 0.f}; lsum[t] = 0.f; }
    {
        int slot = 0;
        for (int st = 0; st < 16; ++st) {
            if (st <= 12) WAIT_BAR(12);
            else if (st == 13) WAIT_BAR(8);
            else if (st == 14) WAIT_BAR(4);
            else WAIT_BAR(0);
            if (st == 0) { issue_kv(2, 2); issue_kv(3, 3); }
            if (st <= 11) issue_kv(st + 4, slot == 0 ? 4 : slot - 1);
            lds_cptr kp = lds + slot * KV_SLOT + hsel * 16384 + row_off;
            lds_cptr vp = lds + slot * KV_SLOT + hsel * 16384 + 8192 + tr_off;
            f32x4 sc[NT][2];
#pragma unroll
            for (int T = 0; T < 2; ++T)
#pragma unroll
                for (int s = 0; s < 4; ++s) {
                    const bf16x8 kf = lds_row_frag(kp + T * 4096 + s * 512);
#pragma unroll
                    for (int t = 0; t < NT; ++t) sc[t][T] = MFMA(kf, qf[t][s], s == 0 ? negm[t] : sc[t][T]);
                }
            u32x4 pf[NT];
            float ps[NT];
            unsigned any = 0;
#pragma unroll
            for (int t = 0; t < NT; ++t) {
                float p[8];
#pragma unroll
                for (int j = 0; j < 8; ++j) p[j] = __builtin_amdgcn_exp2f(sc[t][j >> 2][j & 3]);
                ps[t] = ((p[0] + p[1]) + (p[2] + p[3])) + ((p[4] + p[5]) + (p[6] + p[7]));
                pf[t] = (u32x4){pk_bf16(p[0], p[1]), pk_bf16(p[2], p[3]), pk_bf16(p[4], p[5]), pk_bf16(p[6], p[7])};
                any |= pf[t][0] | pf[t][1] | pf[t][2] | pf[t][3];
            }
            if (__builtin_expect(st == 0 || __any((any & 0x40004000u) != 0), 0)) {
#pragma unroll
                for (int t = 0; t < NT; ++t) {
                    float mx = fmaxf(fmaxf(fmaxf(sc[t][0][0], sc[t][0][1]), fmaxf(sc[t][0][2], sc[t][0][3])),
                                     fmaxf(fmaxf(sc[t][1][0], sc[t][1][1]), fmaxf(sc[t][1][2], sc[t][1][3])));
                    mx = xor32_max(xor16_max(mx));
                    float dl = mx + PBIAS;
                    if (st != 0) dl = fmaxf(dl, 0.f);
                    const float alpha = st == 0 ? 1.0f : __builtin_amdgcn_exp2f(-dl);
                    const float nm = negm[t][0] - dl;
                    negm[t] = (f32x4){nm, nm, nm, nm};
                    lsum[t] *= alpha;
#pragma unroll
                    for (int d = 0; d < 8; ++d) acc[t][d] *= alpha;
                    float p[8];
#pragma unroll
                    for (int j = 0; j < 8; ++j) p[j] = __builtin_amdgcn_exp2f(sc[t][j >> 2][j & 3] - dl);
                    ps[t] = ((p[0] + p[1]) + (p[2] + p[3])) + ((p[4] + p[5]) + (p[6] + p[7]));
                    pf[t] = (u32x4){pk_bf16(p[0], p[1]), pk_bf16(p[2], p[3]), pk_bf16(p[4], p[5]), pk_bf16(p[6], p[7])};
                }
            }
#pragma unroll
            for (int t = 0; t < NT; ++t) lsum[t] += ps[t];
#pragma unroll
            for (int d = 0; d < 8; ++d) {
                const bf16x8 vf = lds_tr_frag(vp + d * 256);
#pragma unroll
                for (int t = 0; t < NT; ++t) acc[t][d] = MFMA(vf, __builtin_bit_cast(bf16x8, pf[t]), acc[t][d]);
            }
            slot = slot == KV_NSLOT - 1 ? 0 : slot + 1;
        }
    }
#pragma unroll
    for (int t = 0; t < NT; ++t) {
        const float inv = 1.0f / xor32_sum(xor16_sum(lsum[t]));
#pragma unroll
        for (int s = 0; s < 4; ++s) {
            const f32x4 u = acc[t][2 * s] * inv, v = acc[t][2 * s + 1] * inv;
            const u32x4 o = {pk_bf16(u[0], u[1]), pk_bf16(u[2], u[3]), pk_bf16(v[0], v[1]), pk_bf16(v[2], v[3])};
            *(u32x4*)(og + (size_t)(s * NQT + qt0 + t) * 1024 + lane * 16) = o;
        }
    }
}

__global__ __launch_bounds__(512, 2) void attn_kernel(const char* xA, const char* kA, const char* vA, const char* wqA, const char* xB,
                                                      const char* kB, const char* vB, const char* wqB, char* ws) {
    extern __shared__ __attribute__((aligned(16))) char smem[];
    const int bid = blockIdx.x, xcd = bid & 7, idx = bid >> 3;
    const int blk = xcd >> 2, b = idx >> 2, h0 = (xcd & 3) * 8 + (idx & 3) * 2;
    const int w = __builtin_amdgcn_readfirstlane(threadIdx.x >> 6);
    if (bid < 64 && threadIdx.x == 0) *(unsigned*)(ws + WS_CNT + bid * 256) = 0u;
    const char* xg = (blk ? xB : xA) + (size_t)b * SQ * DM * 2;
    const char* wg = (blk ? wqB : wqA) + (size_t)(h0 + (w >> 2)) * DH * 2;
    const char* kvbase = ((w >> 1) & 1) ? (blk ? vB : vA) : (blk ? kB : kA);
    const char* kvg = kvbase + ((size_t)(b * NH + h0 + (w >> 2)) * SK + (w & 1) * 16) * (DH * 2);
    char* og = ws + WS_O + (size_t)((blk * NB + b) * NH + h0 + (w >> 2)) * (4 * NQT * 1024);
    const unsigned lds0 = (unsigned)(uintptr_t)smem;
    lds_cptr lds = (lds_cptr)smem;
    if (w == 0 || w == 5) attn_body<3>(xg, wg, kvg, og, w, w == 0 ? 0 : 2, lds, lds0);
    else attn_body<2>(xg, wg, kvg, og, w, w == 4 ? 0 : (w == 1 ? 3 : 1 + 2 * (w & 3)), lds, lds0);
}

constexpr int OP_SLOT = 16384 + 18432, OP_NSLOT = 4, OP_LDS = OP_SLOT * OP_NSLOT;

template <int NT>
__device__ __forceinline__ void oproj_body(const char* wog, const char* ofg, char* part, gu32* cnt, char* outg, int w, int kq, int qt0,
                                           lds_cptr lds, unsigned lds0) {
    const int lane = threadIdx.x & 63, r = lane & 15, g = lane >> 4;
    const int nqt = w & 3;
    const int e = (lane >> 1) & 7, colb = (lane >> 4) * 32 + (lane & 1) * 16;
    const unsigned voff_w = e * (DM * 2) + colb;
    const unsigned voff_o = lane * 16;
    const unsigned w_dst = lds0 + w * 2048, o_dst = lds0 + 16384 + w * 1024;
    const char* wsrc = wog + (size_t)w * 8 * (DM * 2);
    const char* osrc = ofg + w * 1024;
    const int tr_off = (g >> 1) * 2048 + (4 * (g & 1) + (r >> 2)) * 32 + (lane & 3) * 8 + nqt * 512;
    const int o_off = 16384 + qt0 * 1024 + lane * 16;
    auto issue = [&](int st, int slot) {
        const char* so = osrc + (size_t)st * 18432;
        const unsigned d = o_dst + slot * OP_SLOT;
        if (w < 2) glds1(voff_o, so + 16384, d + 16384);
        glds1(voff_o, so, d);
        glds1(voff_o, so + 8192, d + 8192);
        glds2(voff_w, wsrc + (size_t)st * 64 * (DM * 2), w_dst + slot * OP_SLOT);
    };
    issue(0, 0); issue(1, 1); issue(2, 2);
    f32x4 acc[NT][2];
#pragma unroll
    for (int t = 0; t < NT; ++t) { acc[t][0] = (f32x4){0.f, 0.f, 0.f, 0.f}; acc[t][1] = acc[t][0]; }
    for (int st = 0; st < 16; ++st) {
        if (st <= 13) WAIT_BAR(8);
        else if (st == 14) WAIT_BAR(4);
        else WAIT_BAR(0);
        if (st <= 12) issue(st + 3, (st + 3) & 3);
        lds_cptr sp = lds + (st & 3) * OP_SLOT;
#pragma unroll
        for (int ks = 0; ks < 2; ++ks) {
            const bf16x8 w0 = lds_tr_frag(sp + tr_off + ks * 8192), w1 = lds_tr_frag(sp + tr_off + ks * 8192 + 256);
#pragma unroll
            for (int t = 0; t < NT; ++t) {
                const bf16x8 of = lds_row_frag(sp + o_off + ks * 9216 + t * 1024);
                acc[t][0] = MFMA(w0, of, acc[t][0]);
                acc[t][1] = MFMA(w1, of, acc[t][1]);
            }
        }
    }
    const auto rsrc = __builtin_amdgcn_make_buffer_rsrc(part, 0, 16 * 18432, 0x00020000);
    if (nqt != kq) {
        const unsigned base = (nqt * 4 + kq) * 18432 + qt0 * 2048 + lane * 16;
#pragma unroll
        for (int t = 0; t < NT; ++t) {
            __builtin_amdgcn_raw_buffer_store_b128(__builtin_bit_cast(u32x4, acc[t][0]), rsrc, base + t * 2048, 0, 16);
            __builtin_amdgcn_raw_buffer_store_b128(__builtin_bit_cast(u32x4, acc[t][1]), rsrc, base + t * 2048 + 1024, 0, 16);
        }
    }
    asm volatile("s_waitcnt vmcnt(0)\n\ts_barrier" ::: "memory");
    if (threadIdx.x == 0) __hip_atomic_fetch_add(cnt, 1u, __ATOMIC_RELAXED, __HIP_MEMORY_SCOPE_AGENT);
    if (nqt == kq) {
        for (unsigned spins = 0; spins < (1u << 22); ++spins) {
            if (__hip_atomic_load(cnt, __ATOMIC_RELAXED, __HIP_MEMORY_SCOPE_AGENT) >= 4u) break;
            __builtin_amdgcn_s_sleep(1);
        }
        __builtin_amdgcn_fence(__ATOMIC_ACQUIRE, "wavefront");
        u32x4 in[3][NT][2];
#pragma unroll
        for (int j = 0; j < 3; ++j) {
            const int src = (kq + 1 + j) & 3;
            const unsigned base = (nqt * 4 + src) * 18432 + qt0 * 2048 + lane * 16;
#pragma unroll
            for (int t = 0; t < NT; ++t) {
                in[j][t][0] = __builtin_amdgcn_raw_buffer_load_b128(rsrc, base + t * 2048, 0, 16);
                in[j][t][1] = __builtin_amdgcn_raw_buffer_load_b128(rsrc, base + t * 2048 + 1024, 0, 16);
            }
        }
#pragma unroll
        for (int t = 0; t < NT; ++t) {
            f32x4 u = acc[t][0], v = acc[t][1];
#pragma unroll
            for (int j = 0; j < 3; ++j) { u += __builtin_bit_cast(f32x4, in[j][t][0]); v += __builtin_bit_cast(f32x4, in[j][t][1]); }
            const u32x4 o = regroup(pk_bf16(u[0], u[1]), pk_bf16(u[2], u[3]), pk_bf16(v[0], v[1]), pk_bf16(v[2], v[3]));
            *(u32x4*)(outg + (size_t)((qt0 + t) * 16 + r) * (2 * DM * 2) + nqt * 64 + g * 16) = o;
        }
    }
}

__global__ __launch_bounds__(512, 2) void oproj_kernel(const char* woA, const char* woB, char* ws, char* out) {
    extern __shared__ __attribute__((aligned(16))) char smem[];
    const int bid = blockIdx.x, xcd = bid & 7, idx = bid >> 3;
    const int blk = xcd >> 2, kq = xcd & 3, b = idx >> 2, nq = idx & 3;
    const int w = __builtin_amdgcn_readfirstlane(threadIdx.x >> 6);
    const int tile = (blk * NB + b) * 4 + nq;
    const char* wog = (blk ? woB : woA) + (size_t)kq * 1024 * (DM * 2) + nq * 256;
    const char* ofg = ws + WS_O + (size_t)((blk * NB + b) * NH + kq * 8) * (4 * NQT * 1024);
    char* part = ws + WS_PART + (size_t)tile * 16 * 18432;
    gu32* cnt = (gu32*)(ws + WS_CNT + tile * 256);
    char* outg = out + (size_t)b * SQ * (2 * DM * 2) + blk * (DM * 2) + nq * 256;
    const unsigned lds0 = (unsigned)(uintptr_t)smem;
    lds_cptr lds = (lds_cptr)smem;
    if (w < 4) oproj_body<5>(wog, ofg, part, cnt, outg, w, kq, 0, lds, lds0);
    else oproj_body<4>(wog, ofg, part, cnt, outg, w, kq, 5, lds, lds0);
}

extern "C" void kernel_launch(void* const* d_in, const int* in_sizes, int n_in, void* d_out, int out_size, void* d_ws, size_t ws_size,
                              hipStream_t stream) {
    (void)in_sizes; (void)n_in; (void)out_size; (void)ws_size;
    (void)hipFuncSetAttribute((const void*)attn_kernel, hipFuncAttributeMaxDynamicSharedMemorySize, ATTN_LDS);
    (void)hipFuncSetAttribute((const void*)oproj_kernel, hipFuncAttributeMaxDynamicSharedMemorySize, OP_LDS);
    attn_kernel<<<256, 512, ATTN_LDS, stream>>>((const char*)d_in[0], (const char*)d_in[1], (const char*)d_in[2], (const char*)d_in[3],
                                                (const char*)d_in[5], (const char*)d_in[6], (const char*)d_in[7], (const char*)d_in[8],
                                                (char*)d_ws);
    oproj_kernel<<<256, 512, OP_LDS, stream>>>((const char*)d_in[4], (const char*)d_in[9], (char*)d_ws, (char*)d_out);
}
```

```cpp
#include <hip/hip_runtime.h>
#include <hip/hip_bf16.h>
#include <stdint.h>

typedef short bf16x8 __attribute__((ext_vector_type(8)));
typedef short s16x4 __attribute__((ext_vector_type(4)));
typedef float f32x4 __attribute__((ext_vector_type(4)));
typedef float f32x2 __attribute__((ext_vector_type(2)));
typedef __bf16 bf16x2 __attribute__((ext_vector_type(2)));
typedef unsigned u32x4 __attribute__((ext_vector_type(4)));
typedef __attribute__((address_space(3))) const char* lds_cptr;
typedef __attribute__((address_space(1))) unsigned gu32;

constexpr int NB = 8, SQ = 144, DM = 512, NH = 32, SK = 512, DH = 128, NQT = SQ / 16;
constexpr float QSCALE = 0.08838834764831845f * 1.4426950408889634f;
constexpr float PBIAS = 7.0f;

constexpr size_t WS_SYN = 0;
constexpr size_t WS_ACK = 32768;
constexpr size_t WS_O = 65536;
constexpr size_t WS_O_BYTES = (size_t)2 * NB * NH * 4 * NQT * 1024;
constexpr size_t WS_PART = WS_O + WS_O_BYTES;
constexpr int HEAD_O = 4 * NQT * 1024, PART_Q = NQT * 1024;

#define MFMA(a, b, c) __builtin_amdgcn_mfma_f32_16x16x32_bf16(a, b, c, 0, 0, 0)

__device__ __forceinline__ unsigned pk_bf16(float a, float b) {
    f32x2 v = {a, b};
    return __builtin_bit_cast(unsigned, __builtin_convertvector(v, bf16x2));
}
__device__ __forceinline__ bf16x8 lds_row_frag(lds_cptr p) { return *(const __attribute__((address_space(3))) bf16x8*)p; }
__device__ __forceinline__ bf16x8 lds_tr_frag(lds_cptr p) {
    s16x4 lo = __builtin_amdgcn_ds_read_tr16_b64_v4i16((__attribute__((address_space(3))) s16x4*)p);
    s16x4 hi = __builtin_amdgcn_ds_read_tr16_b64_v4i16((__attribute__((address_space(3))) s16x4*)(p + 4096));
    return (bf16x8){lo[0], lo[1], lo[2], lo[3], hi[0], hi[1], hi[2], hi[3]};
}
__device__ __forceinline__ s16x4 lds_tr_half(lds_cptr p) { return __builtin_amdgcn_ds_read_tr16_b64_v4i16((__attribute__((address_space(3))) s16x4*)p); }
__device__ __forceinline__ bf16x8 join(s16x4 lo, s16x4 hi) { return (bf16x8){lo[0], lo[1], lo[2], lo[3], hi[0], hi[1], hi[2], hi[3]}; }
#define GLDS(IMM, voff, sbase, dst) \
    asm volatile("s_mov_b32 m0, %1\n\ts_nop 0\n\tglobal_load_lds_dwordx4 %0, %2 offset:" #IMM ::"v"(voff), "s"((dst) - (IMM)), "s"(sbase) : "memory")
#define GLDS8(POL, v0, v1, v2, v3, v4, v5, v6, v7, sbase, dst) \
    asm volatile("s_mov_b32 m0, %8\n\ts_nop 0\n\t" \
                 "global_load_lds_dwordx4 %0, %9 offset:-4096" POL "\n\tglobal_load_lds_dwordx4 %1, %9 offset:-3072" POL "\n\t" \
                 "global_load_lds_dwordx4 %2, %9 offset:-2048" POL "\n\tglobal_load_lds_dwordx4 %3, %9 offset:-1024" POL "\n\t" \
                 "global_load_lds_dwordx4 %4, %9" POL "\n\tglobal_load_lds_dwordx4 %5, %9 offset:1024" POL "\n\t" \
                 "global_load_lds_dwordx4 %6, %9 offset:2048" POL "\n\tglobal_load_lds_dwordx4 %7, %9 offset:3072" POL \
                 ::"v"(v0), "v"(v1), "v"(v2), "v"(v3), "v"(v4), "v"(v5), "v"(v6), "v"(v7), "s"((dst) + 4096), "s"(sbase) : "memory")
#define SB() __builtin_amdgcn_sched_barrier(0)
#define LGKM0() __builtin_amdgcn_s_waitcnt(0xC07F)
#define BAR_ONLY() asm volatile("s_barrier" ::: "memory")
#define VM_BAR(N) asm volatile("s_waitcnt vmcnt(" #N ")\n\ts_barrier" ::: "memory")
__device__ __forceinline__ u32x4 regroup(unsigned a0, unsigned a1, unsigned b0, unsigned b1) {
    auto r0 = __builtin_amdgcn_permlane32_swap(a0, b0, false, false);
    auto r1 = __builtin_amdgcn_permlane32_swap(a1, b1, false, false);
    auto x0 = __builtin_amdgcn_permlane16_swap(r0[0], r0[1], false, false);
    auto x1 = __builtin_amdgcn_permlane16_swap(r1[0], r1[1], false, false);
    return (u32x4){x0[0], x1[0], x0[1], x1[1]};
}
constexpr int HALF = 16384, RING = 10;
constexpr int W_RING = 4 * HALF, W_SLOT = 16384, X_RING = W_RING + 3 * W_SLOT, X_SLOT = 18432;
constexpr int SPARE = X_RING + 2 * X_SLOT;
constexpr int ATTN_LDS = RING * HALF;
constexpr int CODE_BYTES = 32768;
constexpr int WIN = 40;
constexpr int PACE = 120, PACE0 = 4;
constexpr int TAPER = 3;
constexpr int EARLY = 6;
#define PIN(x) asm volatile("" : "+v"(x))
__device__ __forceinline__ int ring_next(int off, int n) { off += n * HALF; return off >= RING * HALF ? off - RING * HALF : off; }
__device__ __forceinline__ lds_cptr lds_at(unsigned a) { PIN(a); return (lds_cptr)(uintptr_t)a; }

#define RLX __ATOMIC_RELAXED, __HIP_MEMORY_SCOPE_AGENT
__device__ __forceinline__ void exchange_wait(gu32* syn_in, gu32* ack_in, gu32* ack_out, unsigned nonce, unsigned& seen, bool mine, bool serve) {
#pragma unroll 1
    for (unsigned spins = 0; spins < (1u << 20); ++spins) {
        bool ok = true;
        if (serve) {
            const unsigned a = __hip_atomic_load(ack_in, RLX), sy = __hip_atomic_load(syn_in, RLX);
            if (sy != seen) { seen = sy; __hip_atomic_store(ack_out, sy, RLX); }
            ok = !mine || a == nonce;
        }
        if (__all(ok)) break;
        __builtin_amdgcn_s_sleep(1);
    }
}
__device__ __forceinline__ void exchange_close(gu32* syn_in, gu32* ack_out, unsigned seen, bool mine) {
    if (mine) {
        const unsigned sy = __hip_atomic_load(syn_in, RLX);
        if (sy != seen) __hip_atomic_store(ack_out, sy, RLX);
    }
}

__device__ __forceinline__ void attn_loader(const char* wg, const char* kg, const char* vg, const char* wog, char* ws, const char* code,
                                            int hp, int shr, int rot, unsigned lds0) {
    const int lane = threadIdx.x & 63;
    const int e = (lane >> 2) & 7, colb = (lane >> 5) * 64 + (((lane & 3) ^ (((lane >> 4) & 1) << 1)) * 16);
    const unsigned voff_o = e * (DM * 2) + (colb ^ ((blockIdx.x & 1) * 64));
    const unsigned vk0 = e * 256 + colb + 4096, vk1 = vk0 - 896;
    unsigned vw[8], vo[8], vp[8];
#pragma unroll
    for (int p = 0; p < 8; ++p) {
        vw[p] = (8 * (e >> 2) + (e & 3) + 16 * ((p >> 1) & 1) + 4 * (p >> 2)) * (NH * DH * 2) + colb + (p & 1) * 128 + 4096 - p * 1024;
        vo[p] = voff_o + (p >> 1) * 8 * (DM * 2) + (p & 1) * 128 + 4096 - p * 1024;
        vp[p] = voff_o + (p >> 1) * 8 * (DM * 2) + ((p & 1) ^ 1) * 128 + 4096 - p * 1024;
    }
    constexpr size_t head_pitch = (size_t)SK * DH * 2;
    auto issue_w = [&](int i) {
        const char* s = wg + (size_t)((i + 2 * rot) & 15) * 32 * (NH * DH * 2);
        const unsigned d = lds0 + W_RING + ((i + 2) % 3) * W_SLOT;
        GLDS8("", vw[0], vw[1], vw[2], vw[3], vw[4], vw[5], vw[6], vw[7], s, d);
        GLDS8("", vw[0], vw[1], vw[2], vw[3], vw[4], vw[5], vw[6], vw[7], s + 256, d + 8192);
    };
    auto issue_kv = [&](int q) {
        const int seq = q >> 1, hd = (q ^ seq) & 1;
        const char* s = ((seq & 1) ? vg : kg) + hd * head_pitch + (size_t)(seq >> 1) * 8192;
        GLDS8(" nt", vk0, vk1, vk0, vk1, vk0, vk1, vk0, vk1, s, lds0 + (seq % RING) * HALF + hd * 8192);
    };
    auto issue_wo = [&](int i) {
        const int q = i < 4 ? i : ((i - 4) * 11) >> 5, pr = i < 4 ? 0 : i - 3 - 3 * q;
        const char* s = wog + (size_t)((hp ^ pr) * 256 + q * 32) * (DM * 2);
        const unsigned d = lds0 + ((32 + i) % RING) * HALF;
        GLDS8("", vo[0], vo[1], vo[2], vo[3], vo[4], vo[5], vo[6], vo[7], s, d);
        GLDS8("", vp[0], vp[1], vp[2], vp[3], vp[4], vp[5], vp[6], vp[7], s + 128 * (DM * 2), d + 8192);
    };
    const int bid = blockIdx.x;
    const bool m1 = lane < 3, m2 = lane >= 4 && lane < 7, m3 = lane >= 8 && lane < 11;
    const int peer = bid ^ (m2 ? lane - 3 : ((lane + 1) & 3) << 3);
    gu32* syn_in = (gu32*)(ws + WS_SYN + bid * 128) + (lane & 15);
    gu32* ack_in = (gu32*)(ws + WS_ACK + bid * 128) + (lane & 15);
    gu32* ack_out = (gu32*)(ws + WS_ACK + peer * 128) + (lane & 15);
    unsigned nonce;
    issue_kv(0); issue_w(2);
    asm volatile("global_load_dword %0, %1, off sc1" : "=v"(nonce) : "v"(ack_in) : "memory");
    asm volatile("s_mov_b32 m0, %1\n\ts_nop 0\n\tglobal_load_lds_dword %0, off" ::"v"(code), "s"(lds0 + SPARE) : "memory");
    VM_BAR(25);
    BAR_ONLY();
    issue_w(3); issue_kv(1);
    VM_BAR(25);
    issue_w(4);
    asm volatile("s_waitcnt vmcnt(24)" : "+v"(nonce)::"memory");
    nonce += 1u;
    if (m1 || m2 || m3) __hip_atomic_store((gu32*)(ws + WS_SYN + peer * 128) + (lane & 15), nonce, RLX);
#pragma unroll 1
    for (int i = 2; i < 13; ++i) {
        if (i >= 3 && i <= 10) VM_BAR(32); else VM_BAR(24);
        if (i == 11) asm volatile("s_mov_b32 m0, %1\n\ts_nop 0\n\tglobal_load_lds_dword %0, off sc1" ::"v"(syn_in), "s"(lds0 + SPARE + 512) : "memory");
        issue_w(i + 3);
        if (!(i & 1)) {
            issue_kv((i >> 1) + 1);
            if (i <= 8) {
                const unsigned vl = lane * 16 + 4096;
                GLDS8("", vl, vl, vl, vl, vl, vl, vl, vl, wog - hp * 256 + (size_t)(shr * 4 + (i >> 1) - 1) * 8192, lds0 + SPARE + 1024);
            }
        }
    }
    int g = 8;
    unsigned t_pace;
    auto issue_to = [&](int target) {
#pragma unroll 1
        for (; g < target; ++g) {
            asm volatile("s_waitcnt vmcnt(%0)" ::"i"(WIN - 8) : "memory");
            unsigned now;
            while ((int)((now = (unsigned)__builtin_amdgcn_s_memrealtime() * 4u) - t_pace) < 0) __builtin_amdgcn_s_sleep(1);
            t_pace = ((int)(now - PACE0 * PACE - t_pace) > 0 ? now - PACE0 * PACE : t_pace) + PACE;
            issue_kv(g);
        }
    };
    VM_BAR(24);
    t_pace = (unsigned)__builtin_amdgcn_s_memrealtime() * 4u;
    issue_kv(8); issue_kv(9);
    VM_BAR(24);
    unsigned seen = *(const __attribute__((address_space(3))) unsigned*)(uintptr_t)(lds0 + SPARE + 512 + lane * 4);
    issue_kv(10); issue_kv(11);
    g = 12;
    PIN(seen);
    BAR_ONLY();
    issue_to(16);
#pragma unroll 1
    for (int st = 0; st < 14; ++st) {
        VM_BAR(24);
        issue_to(min(64, max(4 * st + 14, min(4 * st + 2 * RING, 2 * RING + TAPER * st))));
    }
#pragma unroll 1
    for (int i = 0; i < 8; ++i) {
        if (i == 2) VM_BAR(40);
        else if (i == 5) VM_BAR(63);
        issue_wo(i);
    }
    VM_BAR(63);
    if (m1) __hip_atomic_store(ack_out, seen, RLX);
    VM_BAR(63);
    VM_BAR(63);
    exchange_wait(syn_in, ack_in, ack_out, nonce, seen, m1, m1);
    BAR_ONLY();
    if (m3) __hip_atomic_store(ack_out, seen, RLX);
    BAR_ONLY();
    unsigned early;
    asm volatile("global_load_dword %0, %1, off sc1" : "=v"(early) : "v"(ack_in) : "memory");
    issue_wo(8);
    BAR_ONLY();
    issue_wo(9);
    asm volatile("s_waitcnt vmcnt(32)" : "+v"(early)::"memory");
    if (!__all(!m3 || early == nonce)) exchange_wait(syn_in, ack_in, ack_out, nonce, seen, m3, m1 || m3);
#pragma unroll 1
    for (int i = 10; i < 16; ++i) {
        VM_BAR(32);
        issue_wo(i);
    }
    VM_BAR(32); VM_BAR(16); VM_BAR(0);
    BAR_ONLY();
    BAR_ONLY();
    BAR_ONLY();
    if (m2) __hip_atomic_store(ack_out, seen, RLX);
    exchange_wait(syn_in, ack_in, ack_out, nonce, seen, m2, m1 || m2 || m3);
    BAR_ONLY();
    exchange_close(syn_in, ack_out, seen, m2);
}

__device__ __forceinline__ float max3(float a, float b, float c) { return __builtin_fmaxf(__builtin_fmaxf(a, b), c); }
__device__ __forceinline__ float col_max(const f32x4& a, const f32x4& b) {
    const float m = max3(max3(a[0], a[1], a[2]), max3(a[3], b[0], b[1]), max3(b[2], b[3], b[3]));
    const auto r = __builtin_amdgcn_permlane16_swap(__float_as_uint(m), __float_as_uint(m), false, false);
    const auto r0 = __builtin_amdgcn_permlane32_swap(r[0], r[1], false, false);
    const auto r1 = __builtin_amdgcn_permlane32_swap(r[1], r[0], false, false);
    return max3(max3(__uint_as_float(r0[0]), __uint_as_float(r0[1]), __uint_as_float(r1[0])), __uint_as_float(r1[1]), __uint_as_float(r1[1]));
}
__device__ __forceinline__ u32x4 exp_pack(const f32x4& a, const f32x4& b) {
    return (u32x4){pk_bf16(__builtin_amdgcn_exp2f(a[0]), __builtin_amdgcn_exp2f(a[1])), pk_bf16(__builtin_amdgcn_exp2f(a[2]), __builtin_amdgcn_exp2f(a[3])),
                   pk_bf16(__builtin_amdgcn_exp2f(b[0]), __builtin_amdgcn_exp2f(b[1])), pk_bf16(__builtin_amdgcn_exp2f(b[2]), __builtin_amdgcn_exp2f(b[3]))};
}

template <int NT>
__device__ __forceinline__ void attn_body(const char* xg, char* ogrp, char* part, char* outg, int hp, int kq, int hsel, int qt0, int xi, int rot,
                                          char* smem) {
    lds_cptr lds = (lds_cptr)smem;
    const unsigned lds0 = (unsigned)(uintptr_t)smem;
    const int lane = threadIdx.x & 63, r = lane & 15, g = lane >> 4;
    const int row_in = (r & 7) * 64 + ((g ^ (((r >> 2) & 1) << 1)) * 16);
    const int row_off = hsel * 8192 + (r >> 3) * 2048 + row_in;
    const int tr_off = hsel * 8192 + (g >> 1) * 2048 + (4 * (g & 1) + (r >> 2)) * 64 + (g & 1) * 32 + (lane & 3) * 8;

    f32x4 acc[NT][8];
#pragma unroll
    for (int t = 0; t < NT; ++t)
#pragma unroll
        for (int d = 0; d < 8; ++d) acc[t][d] = (f32x4){0.f, 0.f, 0.f, 0.f};
    bf16x8 kf[8];

    {
        const int x_off = X_RING + (2 * qt0 + (r >> 3)) * 1024 + row_in;
        const unsigned voff_x = ((lane >> 2) & 7) * (DM * 2) + (lane >> 5) * 64 + (((lane & 3) ^ (((lane >> 4) & 1) << 1)) * 16);
        auto issue_x = [&](int j) {
            const char* s = xg + (size_t)xi * 48 * (DM * 2) + ((j + rot) & 7) * 128;
            const unsigned d = lds0 + X_RING + (j & 1) * X_SLOT + xi * 6144 + 3072, v = voff_x + 3072;
            asm volatile("s_mov_b32 m0, %6\n\ts_nop 0\n\t"
                         "global_load_lds_dwordx4 %0, %7 offset:-3072\n\tglobal_load_lds_dwordx4 %1, %7 offset:-2048\n\t"
                         "global_load_lds_dwordx4 %2, %7 offset:-1024\n\tglobal_load_lds_dwordx4 %3, %7\n\t"
                         "global_load_lds_dwordx4 %4, %7 offset:1024\n\tglobal_load_lds_dwordx4 %5, %7 offset:2048"
                         ::"v"(v), "v"(v + 7168), "v"(v + 2 * 7168), "v"(v + 3 * 7168), "v"(v + 4 * 7168), "v"(v + 5 * 7168), "s"(d), "s"(s) : "memory");
        };
        if (NT == 2) issue_x(1);
#pragma unroll
        for (int t = 0; t < NT; ++t)
#pragma unroll
            for (int d = 0; d < 8; ++d) PIN(acc[t][d]);
        lds_cptr x0p = lds_at(lds0 + x_off), w0p = lds_at(lds0 + W_RING + 2 * W_SLOT + tr_off), w1p = lds_at(lds0 + W_RING + 2 * W_SLOT + (tr_off ^ 32));
        if (NT == 2) VM_BAR(6); else VM_BAR(0);
        bf16x8 wa[8], wb[8], xa[NT], xb[NT];
#pragma unroll
        for (int t = 0; t < NT; ++t) xa[t] = lds_row_frag(x0p + t * 2048);
#pragma unroll
        for (int d = 0; d < 8; ++d) wa[d] = lds_tr_frag(((d & 1) ? w1p : w0p) + (d >> 1) * 512);
        int s_nxt = 0;
#define QREAD(n, WN, XN) do { \
            if ((n) < 16) { hf[(n) & 1] = lds_tr_half((((n) & 2) ? wp1 : wp0) + ((n) >> 2) * 512 + ((n) & 1) * 4096); if ((n) & 1) WN[(n) >> 1] = join(hf[0], hf[1]); } \
            else if ((n) < 16 + NT) XN[(n) - 16] = lds_row_frag(xp + ((n) - 16) * 2048); } while (0)
#define QSTEP(WC, XC, WN, XN, XNO, SYNC) do { \
            lds_cptr wp0 = lds_at(lds0 + W_RING + s_nxt + tr_off), wp1 = lds_at(lds0 + W_RING + s_nxt + (tr_off ^ 32)); \
            lds_cptr xp = lds_at(lds0 + x_off + (XNO)); \
            s16x4 hf[2]; \
            LGKM0(); SYNC; SB(); \
            _Pragma("unroll") for (int n = 0; n < 8 * NT; ++n) { \
                acc[n % NT][n / NT] = MFMA(WC[n / NT], XC[n % NT], acc[n % NT][n / NT]); \
                SB(); \
                QREAD(n, WN, XN); SB(); \
            } \
            if (NT == 2) { XN[0] = lds_row_frag(xp); XN[1] = lds_row_frag(xp + 2048); SB(); } \
            s_nxt = s_nxt == 2 * W_SLOT ? 0 : s_nxt + W_SLOT; } while (0)
#pragma unroll 1
        for (int it = 0; it < 7; ++it) {
            QSTEP(wa, xa, wb, xb, (it & 1) * X_SLOT + 512, BAR_ONLY());
            QSTEP(wb, xb, wa, xa, ((it + 1) & 1) * X_SLOT, if (NT == 2) VM_BAR(0); else BAR_ONLY());
            if (NT == 2 && it < 6) issue_x(it + 2);
        }
        QSTEP(wa, xa, wb, xb, X_SLOT + 512, BAR_ONLY());
        lds_cptr k0p = lds_at(lds0 + row_off);
        LGKM0(); BAR_ONLY(); SB();
#pragma unroll
        for (int d = 0; d < 8; ++d) {
#pragma unroll
            for (int t = 0; t < NT; ++t) acc[t][d] = MFMA(wb[d], xb[t], acc[t][d]);
            kf[d] = lds_row_frag(k0p + (d >> 2) * 4096 + (d & 3) * 512);
            SB();
        }
#undef QSTEP
#undef QREAD
    }
    bf16x8 qf[NT][4];
#pragma unroll
    for (int t = 0; t < NT; ++t)
#pragma unroll
        for (int s = 0; s < 4; ++s) {
            const f32x4 u = acc[t][2 * s] * QSCALE, v = acc[t][2 * s + 1] * QSCALE;
            qf[t][s] = __builtin_bit_cast(bf16x8, regroup(pk_bf16(u[0], u[1]), pk_bf16(u[2], u[3]), pk_bf16(v[0], v[1]), pk_bf16(v[2], v[3])));
        }

    f32x4 negm[NT], lacc[NT], sc[NT][2];
    SB();
#pragma unroll
    for (int n = 0; n < 8 * NT; ++n) {
        const int j = n / NT, t = n % NT;
        sc[t][j >> 2] = MFMA(kf[j], qf[t][j & 3], (j & 3) == 0 ? ((f32x4){0.f, 0.f, 0.f, 0.f}) : sc[t][j >> 2]);
        SB();
        acc[t][j] = (f32x4){0.f, 0.f, 0.f, 0.f};
        PIN(acc[t][j]);
        SB();
    }
#pragma unroll
    for (int t = 0; t < NT; ++t) {
        const float nm = -(col_max(sc[t][0], sc[t][1]) + PBIAS);
        negm[t] = (f32x4){nm, nm, nm, nm};
        sc[t][0] += nm; sc[t][1] += nm;
        lacc[t] = (f32x4){0.f, 0.f, 0.f, 0.f};
    }
    u32x4 ones = {0x3f803f80u, 0x3f803f80u, 0x3f803f80u, 0x3f803f80u};
    PIN(ones);

#define VHALF(n) do { hf[(n) & 1] = lds_tr_half((((n) & 2) ? vp1 : vp0) + ((n) >> 2) * 512 + ((n) & 1) * 4096); if ((n) & 1) vf[(n) >> 1] = join(hf[0], hf[1]); } while (0)
#define KREAD(j) kf[j] = lds_row_frag(kp + ((j) >> 2) * 4096 + ((j) & 3) * 512)
#define PFRAG(t) __builtin_bit_cast(bf16x8, pf[t])
#define ONES __builtin_bit_cast(bf16x8, ones)
#define RESCALE_IF_NEEDED(kc) do { \
        unsigned any = 0; \
        _Pragma("unroll") for (int t = 0; t < NT; ++t) { any |= pf[t][0] | pf[t][1]; any |= pf[t][2] | pf[t][3]; } \
        if (__builtin_expect(__any((any & 0x40004000u) != 0), 0)) { \
            _Pragma("unroll") for (int t = 0; t < NT; ++t) { \
                f32x4 s0 = negm[t], s1 = negm[t]; \
                _Pragma("unroll") for (int j = 0; j < 4; ++j) { \
                    s0 = MFMA(lds_row_frag((kc) + j * 512), qf[t][j], s0); SB(); \
                    s1 = MFMA(lds_row_frag((kc) + 4096 + j * 512), qf[t][j], s1); SB(); } \
                const float dl = fmaxf(col_max(s0, s1) + PBIAS, 0.f); \
                const float alpha = __builtin_amdgcn_exp2f(-dl); \
                negm[t] -= dl; \
                lacc[t] *= alpha; \
                _Pragma("unroll") for (int d = 0; d < 8; ++d) acc[t][d] *= alpha; \
                pf[t] = exp_pack(s0 - dl, s1 - dl); \
                SB(); \
            } \
        } \
        SB(); } while (0)
    bf16x8 vf[8];
    s16x4 hf[2];
    u32x4 pf[NT];
    if (NT == 3) {
        int p_k = 0;
#pragma unroll 1
        for (int st = 0; st < 16; ++st) {
            const int p_c = p_k, p_v = p_k + HALF;
            p_k = ring_next(p_k, 2);
            lds_cptr vp0 = lds_at(lds0 + p_v + tr_off), vp1 = lds_at(lds0 + p_v + (tr_off ^ 32));
            lds_cptr kp = lds + p_k + row_off;
            __builtin_amdgcn_s_setprio(2);
            LGKM0(); BAR_ONLY();
            SB();
#pragma unroll
            for (int n = 0; n < 24; ++n) {
                const int j = n / 3, t = n % 3;
                sc[t][j >> 2] = MFMA(kf[j], qf[t][j & 3], (j & 3) == 0 ? negm[t] : sc[t][j >> 2]);
                SB();
                if (t != 1 && n >= 2) VHALF(t == 2 ? 2 * j : 2 * j - 1);
                if (n >= 14 && n - 14 < EARLY) { float e_ = __builtin_amdgcn_exp2f(sc[(n - 14) >> 2][0][(n - 14) & 3]); PIN(e_); sc[(n - 14) >> 2][0][(n - 14) & 3] = e_; }
                SB();
            }
            VHALF(15);
#pragma unroll
            for (int i = EARLY; i < 12; ++i) sc[i >> 2][0][i & 3] = __builtin_amdgcn_exp2f(sc[i >> 2][0][i & 3]);
#pragma unroll
            for (int t = 0; t < NT; ++t) {
                pf[t][0] = pk_bf16(sc[t][0][0], sc[t][0][1]); pf[t][1] = pk_bf16(sc[t][0][2], sc[t][0][3]);
                pf[t][2] = pk_bf16(__builtin_amdgcn_exp2f(sc[t][1][0]), __builtin_amdgcn_exp2f(sc[t][1][1]));
                pf[t][3] = pk_bf16(__builtin_amdgcn_exp2f(sc[t][1][2]), __builtin_amdgcn_exp2f(sc[t][1][3]));
            }
            RESCALE_IF_NEEDED(lds + p_c + row_off);
            __builtin_amdgcn_s_setprio(0);
#pragma unroll
            for (int n = 0; n < 24; ++n) {
                acc[n % 3][n / 3] = MFMA(vf[n / 3], PFRAG(n % 3), acc[n % 3][n / 3]);
                SB();
                if (n < 16 && (n & 1)) { KREAD(n >> 1); SB(); }
            }
#pragma unroll
            for (int t = 0; t < NT; ++t) lacc[t] = MFMA(ONES, PFRAG(t), lacc[t]);
            SB();
        }
    } else {
        {
            lds_cptr vp0 = lds + HALF + tr_off, vp1 = lds + HALF + (tr_off ^ 32);
#pragma unroll
            for (int n = 0; n < 16; ++n) VHALF(n);
#pragma unroll
            for (int t = 0; t < NT; ++t) pf[t] = exp_pack(sc[t][0], sc[t][1]);
        }
        int p_k = 0;
#pragma unroll 1
        for (int st = 0; st < 15; ++st) {
            p_k = ring_next(p_k, 2);
            lds_cptr vp0 = lds_at(lds0 + p_k + HALF + tr_off), vp1 = lds_at(lds0 + p_k + HALF + (tr_off ^ 32));
            lds_cptr kp = lds_at(lds0 + p_k + row_off);
            LGKM0(); BAR_ONLY();
            SB();
#pragma unroll
            for (int n = 0; n < 16; ++n) {
                acc[n & 1][n >> 1] = MFMA(vf[n >> 1], PFRAG(n & 1), acc[n & 1][n >> 1]);
                SB();
                if (n < 8) { KREAD(n); SB(); }
            }
#pragma unroll
            for (int t = 0; t < NT; ++t) lacc[t] = MFMA(ONES, PFRAG(t), lacc[t]);
            SB();
#pragma unroll
            for (int n = 0; n < 16; ++n) {
                const int j = n >> 1, t = n & 1;
                sc[t][j >> 2] = MFMA(kf[j], qf[t][j & 3], (j & 3) == 0 ? negm[t] : sc[t][j >> 2]);
                SB();
                VHALF(n);
                SB();
            }
#pragma unroll
            for (int t = 0; t < NT; ++t) pf[t] = exp_pack(sc[t][0], sc[t][1]);
            RESCALE_IF_NEEDED(kp);
        }
        BAR_ONLY();
#pragma unroll
        for (int n = 0; n < 16; ++n) acc[n & 1][n >> 1] = MFMA(vf[n >> 1], PFRAG(n & 1), acc[n & 1][n >> 1]);
#pragma unroll
        for (int t = 0; t < NT; ++t) lacc[t] = MFMA(ONES, PFRAG(t), lacc[t]);
    }
#undef RESCALE_IF_NEEDED
#undef ONES
#undef PFRAG
#undef KREAD
#undef VHALF
    unsigned lane2;
    asm volatile("v_mbcnt_lo_u32_b32 %0, -1, 0\n\tv_mbcnt_hi_u32_b32 %0, -1, %0" : "=v"(lane2));
    const auto orsrc = __builtin_amdgcn_make_buffer_rsrc(ogrp + hp * 2 * HEAD_O, 0, 2 * HEAD_O, 0x00020000);
    const unsigned o_off = hsel * HEAD_O + qt0 * 1024 + lane2 * 16;
    unsigned o_st[4];
#pragma unroll
    for (int s = 0; s < 4; ++s) { o_st[s] = o_off + s * NQT * 1024; PIN(o_st[s]); }
    u32x4 of[4][NT];
    float inv[NT];
#pragma unroll
    for (int t = 0; t < NT; ++t) inv[t] = __builtin_amdgcn_rcpf(lacc[t][0]);
#define OSTORE(s) do { _Pragma("unroll") for (int t = 0; t < NT; ++t) \
        __builtin_amdgcn_raw_buffer_store_b128(of[s][t], orsrc, o_st[s] + t * 1024, 0, 16); SB(); } while (0)
    bf16x8 wa[8], wb[8];
#pragma unroll
    for (int s = 0; s < 4; ++s) {
#pragma unroll
        for (int t = 0; t < NT; ++t) {
            const f32x4 u = acc[t][2 * s] * inv[t], v = acc[t][2 * s + 1] * inv[t];
            of[s][t] = (u32x4){pk_bf16(u[0], u[1]), pk_bf16(u[2], u[3]), pk_bf16(v[0], v[1]), pk_bf16(v[2], v[3])};
            PIN(of[s][t]);
        }
        if (s < 2) OSTORE(s);
        if (s == 1) {
#pragma unroll
            for (int d = 0; d < 8; ++d) wa[d] = lds_tr_frag(lds + (32 % RING) * HALF + ((d & 1) ? (tr_off ^ 32) : tr_off) + (d >> 1) * 512);
            SB();
        }
    }
    __builtin_amdgcn_s_setprio(NT == 2 ? 1 : 0);
#define OLOAD(s, step) do { const int j_ = (step) - 4, q_ = (j_ * 11) >> 5; \
        const char* b_ = ogrp + ((hp ^ (1 + j_ - 3 * q_)) & 3) * 2 * HEAD_O + (q_ & 3) * NQT * 1024; \
        if (NT == 3) asm volatile("s_cmp_ge_u32 %5, 12\n\ts_cbranch_scc1 1f\n\tglobal_load_dwordx4 %0, %3, %4 sc1\n\tglobal_load_dwordx4 %1, %3, %4 offset:1024 sc1\n\t" \
                                  "global_load_dwordx4 %2, %3, %4 offset:2048 sc1\n1:" \
                                  : "+v"(of[s][0]), "+v"(of[s][1]), "+v"(of[s][NT - 1]) : "v"(o_off), "s"(b_), "s"(j_) : "memory", "scc"); \
        else asm volatile("s_cmp_ge_u32 %4, 12\n\ts_cbranch_scc1 1f\n\tglobal_load_dwordx4 %0, %2, %3 sc1\n\tglobal_load_dwordx4 %1, %2, %3 offset:1024 sc1\n1:" \
                          : "+v"(of[s][0]), "+v"(of[s][1]) : "v"(o_off), "s"(b_), "s"(j_) : "memory", "scc"); } while (0)
#define OWAIT(s) do { \
        if (NT == 3) asm volatile("s_waitcnt vmcnt(%4)\n\ts_cmp_lg_u32 %3, 3\n\ts_cbranch_scc1 1f\n\ts_waitcnt vmcnt(%5)\n1:" \
                                  : "+v"(of[s][0]), "+v"(of[s][1]), "+v"(of[s][NT - 1]) : "s"(ph), "i"(3 * NT), "i"((3 - (s)) * NT) : "memory", "scc"); \
        else asm volatile("s_waitcnt vmcnt(%3)\n\ts_cmp_lg_u32 %2, 3\n\ts_cbranch_scc1 1f\n\ts_waitcnt vmcnt(%4)\n1:" \
                          : "+v"(of[s][0]), "+v"(of[s][1]) : "s"(ph), "i"(3 * NT), "i"((3 - (s)) * NT) : "memory", "scc"); } while (0)
#define OSTEP(step, s, WC, WN, FIRST, SYNC, TAIL) do { \
        en = ring_next(en, 1); \
        lds_cptr wp0 = lds_at(lds0 + en + tr_off), wp1 = lds_at(lds0 + en + (tr_off ^ 32)); \
        LGKM0(); \
        SYNC; \
        SB(); \
        _Pragma("unroll") for (int n = 0; n < 8 * NT; ++n) { \
            acc[n % NT][n / NT] = MFMA(WC[n / NT], __builtin_bit_cast(bf16x8, of[s][n % NT]), FIRST ? ((f32x4){0.f, 0.f, 0.f, 0.f}) : acc[n % NT][n / NT]); \
            SB(); \
            if (n < 16) { hf[n & 1] = lds_tr_half(((n & 2) ? wp1 : wp0) + (n >> 2) * 512 + (n & 1) * 4096); if (n & 1) WN[n >> 1] = join(hf[0], hf[1]); SB(); } \
        } \
        TAIL; \
        SB(); } while (0)
    int en = (32 % RING) * HALF;
    OSTEP(0, 0, wa, wb, true, VM_BAR(0); if (NT == 3) { OSTORE(2); OSTORE(3); }, if (NT == 2) { OSTORE(2); OSTORE(3); });
    OSTEP(1, 1, wb, wa, false, BAR_ONLY(), );
    OSTEP(2, 2, wa, wb, false, BAR_ONLY(), );
    OSTEP(3, 3, wb, wa, false, VM_BAR(0); OLOAD(0, 4); OLOAD(1, 5); OLOAD(2, 6), if (NT == 2) OLOAD(3, 7));
#define OSTEP_P(s, WC, WN) OSTEP(4 * ph + (s), s, WC, WN, false, \
        if (NT == 2) OWAIT(s); BAR_ONLY(); if (NT == 3) { OLOAD(((s) + 3) & 3, 4 * ph + (s) + 3); OWAIT(s); }, if (NT == 2) OLOAD(s, 4 * ph + (s) + 4))
#pragma unroll 1
    for (int ph = 1; ph < 4; ++ph) {
        OSTEP_P(0, wa, wb); OSTEP_P(1, wb, wa); OSTEP_P(2, wa, wb); OSTEP_P(3, wb, wa);
    }
#undef OSTEP_P
#undef OSTORE
    LGKM0();
#undef OWAIT
#undef OSTEP
#undef OLOAD
    const int yq = (2 * hsel) ^ (kq & 1);
    {
        char* red = smem + qt0 * 4096 + lane2 * 16;
#pragma unroll
        for (int t = 0; t < NT; ++t)
#pragma unroll
            for (int c = 0; c < 4; ++c) *(f32x4*)(red + hsel * (NQT * 4096) + t * 4096 + c * 1024) = acc[t][4 + c];
        LGKM0(); BAR_ONLY();
#pragma unroll
        for (int t = 0; t < NT; ++t)
#pragma unroll
            for (int c = 0; c < 4; ++c) acc[t][c] += *(const f32x4*)(red + (hsel ^ 1) * (NQT * 4096) + t * 4096 + c * 1024);
    }
    const auto prsrc = __builtin_amdgcn_make_buffer_rsrc(part, 0, 16 * PART_Q, 0x00020000);
    const unsigned p_off = qt0 * 1024 + lane2 * 16;
#pragma unroll
    for (int u = 1; u >= 0; --u)
        if (u == 1 || yq != kq) {
            const auto dst = __builtin_amdgcn_make_buffer_rsrc(part + ((yq ^ u) * 4 + kq) * PART_Q, 0, PART_Q, 0x00020000);
#pragma unroll
            for (int t = 0; t < NT; ++t) {
                const f32x4 a = acc[t][2 * u], c = acc[t][2 * u + 1];
                const u32x4 pk = {pk_bf16(a[0], a[1]), pk_bf16(a[2], a[3]), pk_bf16(c[0], c[1]), pk_bf16(c[2], c[3])};
                __builtin_amdgcn_raw_buffer_store_b128(pk, dst, p_off + t * 1024, 0, 16);
            }
        }
    VM_BAR(0);
    BAR_ONLY();
    if (yq == kq) {
        u32x4 in[3][NT];
#pragma unroll
        for (int j = 0; j < 3; ++j)
#pragma unroll
            for (int t = 0; t < NT; ++t) in[j][t] = __builtin_amdgcn_raw_buffer_load_b128(prsrc, p_off + t * 1024, (kq * 4 + (kq ^ (j + 1))) * PART_Q, 16);
        const auto outr = __builtin_amdgcn_make_buffer_rsrc(outg + kq * 64, 0, (SQ - 1) * (2 * DM * 2) + 64, 0x00020000);
#pragma unroll
        for (int t = 0; t < NT; ++t) {
            f32x4 u = acc[t][0], v = acc[t][1];
#pragma unroll
            for (int j = 0; j < 3; ++j) {
                const u32x4 x = in[j][t];
                u += (f32x4){__uint_as_float(x[0] << 16), __uint_as_float(x[0] & 0xffff0000u), __uint_as_float(x[1] << 16), __uint_as_float(x[1] & 0xffff0000u)};
                v += (f32x4){__uint_as_float(x[2] << 16), __uint_as_float(x[2] & 0xffff0000u), __uint_as_float(x[3] << 16), __uint_as_float(x[3] & 0xffff0000u)};
            }
            const u32x4 o = regroup(pk_bf16(u[0], u[1]), pk_bf16(u[2], u[3]), pk_bf16(v[0], v[1]), pk_bf16(v[2], v[3]));
            __builtin_amdgcn_raw_buffer_store_b128(o, outr, ((qt0 + t) * 16 + (lane2 & 15)) * (2 * DM * 2) + (lane2 >> 4) * 16, 0, 0);
        }
    }
}

__global__ __launch_bounds__(512, 2) void attn_kernel(const char* xA, const char* kA, const char* vA, const char* wqA, const char* woA,
                                                      const char* xB, const char* kB, const char* vB, const char* wqB, const char* woB,
                                                      char* ws, char* out) {
    extern __shared__ __attribute__((aligned(16))) char smem[];
    unsigned long long pc;
    asm volatile("s_getpc_b64 %0" : "=s"(pc));
    const int bid = blockIdx.x, blk = (bid >> 2) & 1, kq = bid & 3, hp = (bid >> 3) & 3, b = bid >> 5;
    const int h0 = kq * 8 + hp * 2;
    const int w = __builtin_amdgcn_readfirstlane(threadIdx.x >> 6);
    const int rot = (b + 2 * hp) & 4;
    const char* xg = (blk ? xB : xA) + (size_t)b * SQ * DM * 2;
    const char* wg = (blk ? wqB : wqA) + (size_t)h0 * DH * 2;
    {
        const int lane = threadIdx.x & 63, e = (lane >> 2) & 7, colb = (lane >> 5) * 64 + (((lane & 3) ^ (((lane >> 4) & 1) << 1)) * 16);
        const unsigned lds0 = (unsigned)(uintptr_t)smem;
        const char* sw = wg + (size_t)(2 * rot * 32 + 16 * ((w >> 1) & 1) + 4 * (w >> 2)) * (NH * DH * 2) + (w & 1) * 256;
        const unsigned dw = lds0 + W_RING + 2 * W_SLOT + (w >> 1) * 2048 + (w & 1) * 8192;
        const unsigned vw = (8 * (e >> 2) + (e & 3)) * (NH * DH * 2) + colb, vx = e * (DM * 2) + colb;
        GLDS(0, vw, sw, dw); GLDS(128, vw, sw, dw + 1024);
        const char* sx = xg + (size_t)w * 8 * (DM * 2) + rot * 128;
        GLDS(0, vx, sx, lds0 + X_RING + w * 1024);
        GLDS(0, vx, sx + 64 * (DM * 2), lds0 + X_RING + w * 1024 + 8192);
        if (w < 2) GLDS(0, vx, sx + 128 * (DM * 2), lds0 + X_RING + w * 1024 + 16384);
        GLDS(0, vw, sw + 32 * (NH * DH * 2), dw - 2 * W_SLOT); GLDS(128, vw, sw + 32 * (NH * DH * 2), dw - 2 * W_SLOT + 1024);
    }
    char* ogrp = ws + WS_O + (size_t)((blk * NB + b) * NH + kq * 8) * HEAD_O;
    char* part = ws + WS_PART + (size_t)((blk * NB + b) * 4 + hp) * 16 * PART_Q;
    char* outg = out + (size_t)b * SQ * (2 * DM * 2) + blk * (DM * 2) + hp * 256;
    if (w == 0) {
        const char* kg = (blk ? kB : kA) + (size_t)(b * NH + h0) * SK * (DH * 2);
        const char* vg = (blk ? vB : vA) + (size_t)(b * NH + h0) * SK * (DH * 2);
        const char* wog = (blk ? woB : woA) + (size_t)kq * 1024 * (DM * 2) + hp * 256;
        const char* code = (const char*)(pc & ~255ull) + min((hp * 2 + (b & 1)) * 8192 + (int)threadIdx.x * 128, CODE_BYTES - 128);
        __builtin_amdgcn_s_setprio(3);
        attn_loader(wg, kg, vg, wog, ws, code, hp, bid >> 3, rot, (unsigned)(uintptr_t)smem);
    } else if (w <= 4) attn_body<3>(xg, ogrp, part, outg, hp, kq, w >> 2, w == 4 ? 0 : 3 * (w - 1), 0, rot, smem);
    else {
        __builtin_amdgcn_s_setprio(1);
        attn_body<2>(xg, ogrp, part, outg, hp, kq, 1, 2 * w - 7, w - 5, rot, smem);
    }
}

extern "C" void kernel_launch(void* const* d_in, const int* in_sizes, int n_in, void* d_out, int out_size, void* d_ws, size_t ws_size,
                              hipStream_t stream) {
    (void)in_sizes; (void)n_in; (void)out_size; (void)ws_size;
    (void)hipFuncSetAttribute((const void*)attn_kernel, hipFuncAttributeMaxDynamicSharedMemorySize, ATTN_LDS);
    attn_kernel<<<256, 512, ATTN_LDS, stream>>>((const char*)d_in[0], (const char*)d_in[1], (const char*)d_in[2], (const char*)d_in[3],
                                                (const char*)d_in[4], (const char*)d_in[5], (const char*)d_in[6], (const char*)d_in[7],
                                                (const char*)d_in[8], (const char*)d_in[9], (char*)d_ws, (char*)d_out);
}
```

```cpp
#include <hip/hip_runtime.h>
#include <hip/hip_bf16.h>
#include <stdint.h>

typedef short bf16x8 __attribute__((ext_vector_type(8)));
typedef short s16x4 __attribute__((ext_vector_type(4)));
typedef float f32x4 __attribute__((ext_vector_type(4)));
typedef float f32x2 __attribute__((ext_vector_type(2)));
typedef __bf16 bf16x2 __attribute__((ext_vector_type(2)));
typedef unsigned u32x4 __attribute__((ext_vector_type(4)));
typedef __attribute__((address_space(3))) const char* lds_cptr;
typedef __attribute__((address_space(1))) unsigned gu32;

constexpr int NB = 8, SQ = 144, DM = 512, NH = 32, SK = 512, DH = 128, NQT = SQ / 16;
constexpr float QSCALE = 0.08838834764831845f * 1.4426950408889634f;
constexpr float PBIAS = 7.0f;

constexpr size_t WS_SYN = 0;
constexpr size_t WS_ACK = 32768;
constexpr size_t WS_O = 65536;
constexpr size_t WS_O_BYTES = (size_t)2 * NB * NH * 4 * NQT * 1024;
constexpr size_t WS_PART = WS_O + WS_O_BYTES;
constexpr int HEAD_O = 4 * NQT * 1024, PART_Q = NQT * 1024;

#define MFMA(a, b, c) __builtin_amdgcn_mfma_f32_16x16x32_bf16(a, b, c, 0, 0, 0)

__device__ __forceinline__ unsigned pk_bf16(float a, float b) {
    f32x2 v = {a, b};
    return __builtin_bit_cast(unsigned, __builtin_convertvector(v, bf16x2));
}
__device__ __forceinline__ bf16x8 lds_row_frag(lds_cptr p) { return *(const __attribute__((address_space(3))) bf16x8*)p; }
__device__ __forceinline__ bf16x8 lds_tr_frag(lds_cptr p) {
    s16x4 lo = __builtin_amdgcn_ds_read_tr16_b64_v4i16((__attribute__((address_space(3))) s16x4*)p);
    s16x4 hi = __builtin_amdgcn_ds_read_tr16_b64_v4i16((__attribute__((address_space(3))) s16x4*)(p + 4096));
    return (bf16x8){lo[0], lo[1], lo[2], lo[3], hi[0], hi[1], hi[2], hi[3]};
}
__device__ __forceinline__ s16x4 lds_tr_half(lds_cptr p) { return __builtin_amdgcn_ds_read_tr16_b64_v4i16((__attribute__((address_space(3))) s16x4*)p); }
__device__ __forceinline__ bf16x8 join(s16x4 lo, s16x4 hi) { return (bf16x8){lo[0], lo[1], lo[2], lo[3], hi[0], hi[1], hi[2], hi[3]}; }
#define GLDS(IMM, voff, sbase, dst) \
    asm volatile("s_mov_b32 m0, %1\n\ts_nop 0\n\tglobal_load_lds_dwordx4 %0, %2 offset:" #IMM ::"v"(voff), "s"((dst) - (IMM)), "s"(sbase) : "memory")
#define GLDS8(POL, v0, v1, v2, v3, v4, v5, v6, v7, sbase, dst) \
    asm volatile("s_mov_b32 m0, %8\n\ts_nop 0\n\t" \
                 "global_load_lds_dwordx4 %0, %9 offset:-4096" POL "\n\tglobal_load_lds_dwordx4 %1, %9 offset:-3072" POL "\n\t" \
                 "global_load_lds_dwordx4 %2, %9 offset:-2048" POL "\n\tglobal_load_lds_dwordx4 %3, %9 offset:-1024" POL "\n\t" \
                 "global_load_lds_dwordx4 %4, %9" POL "\n\tglobal_load_lds_dwordx4 %5, %9 offset:1024" POL "\n\t" \
                 "global_load_lds_dwordx4 %6, %9 offset:2048" POL "\n\tglobal_load_lds_dwordx4 %7, %9 offset:3072" POL \
                 ::"v"(v0), "v"(v1), "v"(v2), "v"(v3), "v"(v4), "v"(v5), "v"(v6), "v"(v7), "s"((dst) + 4096), "s"(sbase) : "memory")
#define SB() __builtin_amdgcn_sched_barrier(0)
#define LGKM0() __builtin_amdgcn_s_waitcnt(0xC07F)
#define BAR_ONLY() asm volatile("s_barrier" ::: "memory")
#define VM_BAR(N) asm volatile("s_waitcnt vmcnt(" #N ")\n\ts_barrier" ::: "memory")
__device__ __forceinline__ u32x4 regroup(unsigned a0, unsigned a1, unsigned b0, unsigned b1) {
    auto r0 = __builtin_amdgcn_permlane32_swap(a0, b0, false, false);
    auto r1 = __builtin_amdgcn_permlane32_swap(a1, b1, false, false);
    auto x0 = __builtin_amdgcn_permlane16_swap(r0[0], r0[1], false, false);
    auto x1 = __builtin_amdgcn_permlane16_swap(r1[0], r1[1], false, false);
    return (u32x4){x0[0], x1[0], x0[1], x1[1]};
}
constexpr int HALF = 16384, RING = 10;
constexpr int W_RING = 4 * HALF, W_SLOT = 16384, X_RING = W_RING + 3 * W_SLOT, X_SLOT = 18432;
constexpr int SPARE = X_RING + 2 * X_SLOT;
constexpr int ATTN_LDS = RING * HALF;
constexpr int CODE_BYTES = 32768;
constexpr unsigned RAMP = 0x3a3e464cu;
constexpr int WIN = 24;
constexpr int TAPER = 28;
constexpr int EARLY = 6;
#define PIN(x) asm volatile("" : "+v"(x))
__device__ __forceinline__ int ring_next(int off, int n) { off += n * HALF; return off >= RING * HALF ? off - RING * HALF : off; }
__device__ __forceinline__ lds_cptr lds_at(unsigned a) { PIN(a); return (lds_cptr)(uintptr_t)a; }

#define RLX __ATOMIC_RELAXED, __HIP_MEMORY_SCOPE_AGENT
__device__ __forceinline__ void exchange_wait(gu32* syn_in, gu32* ack_in, gu32* ack_out, unsigned nonce, unsigned& seen, bool mine, bool serve) {
#pragma unroll 1
    for (unsigned spins = 0; spins < (1u << 20); ++spins) {
        bool ok = true;
        if (serve) {
            const unsigned a = __hip_atomic_load(ack_in, RLX), sy = __hip_atomic_load(syn_in, RLX);
            if (sy != seen) { seen = sy; __hip_atomic_store(ack_out, sy, RLX); }
            ok = !mine || a == nonce;
        }
        if (__all(ok)) break;
        __builtin_amdgcn_s_sleep(1);
    }
}
__device__ __forceinline__ void exchange_close(gu32* syn_in, gu32* ack_out, unsigned seen, bool mine) {
    if (mine) {
        const unsigned sy = __hip_atomic_load(syn_in, RLX);
        if (sy != seen) __hip_atomic_store(ack_out, sy, RLX);
    }
}

__device__ __forceinline__ void attn_loader(const char* wg, const char* kg, const char* vg, const char* wog, char* ws, const char* code,
                                            int hp, int shr, int rot, unsigned lds0) {
    const int lane = threadIdx.x & 63;
    const int e = (lane >> 2) & 7, colb = (lane >> 5) * 64 + (((lane & 3) ^ (((lane >> 4) & 1) << 1)) * 16);
    const unsigned voff_o = e * (DM * 2) + (colb ^ ((blockIdx.x & 1) * 64));
    const unsigned vk0 = e * 256 + colb + 4096, vk1 = vk0 - 896;
    unsigned vw[8], vo[8], vp[8];
#pragma unroll
    for (int p = 0; p < 8; ++p) {
        vw[p] = (8 * (e >> 2) + (e & 3) + 16 * ((p >> 1) & 1) + 4 * (p >> 2)) * (NH * DH * 2) + colb + (p & 1) * 128 + 4096 - p * 1024;
        vo[p] = voff_o + (p >> 1) * 8 * (DM * 2) + (p & 1) * 128 + 4096 - p * 1024;
        vp[p] = voff_o + (p >> 1) * 8 * (DM * 2) + ((p & 1) ^ 1) * 128 + 4096 - p * 1024;
    }
    constexpr size_t head_pitch = (size_t)SK * DH * 2;
    auto issue_w = [&](int i) {
        const char* s = wg + (size_t)((i + 2 * rot) & 15) * 32 * (NH * DH * 2);
        const unsigned d = lds0 + W_RING + ((i + 2) % 3) * W_SLOT;
        GLDS8("", vw[0], vw[1], vw[2], vw[3], vw[4], vw[5], vw[6], vw[7], s, d);
        GLDS8("", vw[0], vw[1], vw[2], vw[3], vw[4], vw[5], vw[6], vw[7], s + 256, d + 8192);
    };
    auto issue_kv = [&](int q) {
        const int seq = q >> 1, hd = (q ^ seq) & 1;
        const char* s = ((seq & 1) ? vg : kg) + hd * head_pitch + (size_t)(seq >> 1) * 8192;
        GLDS8(" nt", vk0, vk1, vk0, vk1, vk0, vk1, vk0, vk1, s, lds0 + (seq % RING) * HALF + hd * 8192);
    };
    auto issue_wo = [&](int i) {
        const int q = i < 4 ? i : ((i - 4) * 11) >> 5, pr = i < 4 ? 0 : i - 3 - 3 * q;
        const char* s = wog + (size_t)((hp ^ pr) * 256 + q * 32) * (DM * 2);
        const unsigned d = lds0 + ((32 + i) % RING) * HALF;
        GLDS8("", vo[0], vo[1], vo[2], vo[3], vo[4], vo[5], vo[6], vo[7], s, d);
        GLDS8("", vp[0], vp[1], vp[2], vp[3], vp[4], vp[5], vp[6], vp[7], s + 128 * (DM * 2), d + 8192);
    };
    const int bid = blockIdx.x;
    const bool m1 = lane < 3, m2 = lane >= 4 && lane < 7, m3 = lane >= 8 && lane < 11;
    const int peer = bid ^ (m2 ? lane - 3 : ((lane + 1) & 3) << 3);
    gu32* syn_in = (gu32*)(ws + WS_SYN + bid * 128) + (lane & 15);
    gu32* ack_in = (gu32*)(ws + WS_ACK + bid * 128) + (lane & 15);
    gu32* ack_out = (gu32*)(ws + WS_ACK + peer * 128) + (lane & 15);
    unsigned nonce;
    issue_w(1); issue_kv(0); issue_w(2);
    asm volatile("global_load_dword %0, %1, off sc1" : "=v"(nonce) : "v"(ack_in) : "memory");
    asm volatile("s_mov_b32 m0, %1\n\ts_nop 0\n\tglobal_load_lds_dword %0, off" ::"v"(code), "s"(lds0 + SPARE) : "memory");
    VM_BAR(41);
    VM_BAR(25);
    unsigned t_step = (unsigned)__builtin_amdgcn_s_memrealtime();
    auto hold = [&](int i) {
        while ((unsigned)__builtin_amdgcn_s_memrealtime() - t_step < ((RAMP >> (8 * i)) & 255u)) __builtin_amdgcn_s_sleep(1);
    };
    issue_w(3); issue_kv(1);
    hold(0);
    VM_BAR(25);
    t_step = (unsigned)__builtin_amdgcn_s_memrealtime();
    issue_w(4);
    asm volatile("s_waitcnt vmcnt(24)" : "+v"(nonce)::"memory");
    nonce += 1u;
    if (m1 || m2 || m3) __hip_atomic_store((gu32*)(ws + WS_SYN + peer * 128) + (lane & 15), nonce, RLX);
#pragma unroll 1
    for (int i = 2; i < 13; ++i) {
        if (i <= 4) hold(i - 1);
        if (i >= 3 && i <= 10) VM_BAR(32); else VM_BAR(24);
        if (i < 4) t_step = (unsigned)__builtin_amdgcn_s_memrealtime();
        if (i == 11) asm volatile("s_mov_b32 m0, %1\n\ts_nop 0\n\tglobal_load_lds_dword %0, off sc1" ::"v"(syn_in), "s"(lds0 + SPARE + 512) : "memory");
        issue_w(i + 3);
        if (!(i & 1)) {
            issue_kv((i >> 1) + 1);
            if (i <= 8) {
                const unsigned vl = lane * 16 + 4096;
                GLDS8("", vl, vl, vl, vl, vl, vl, vl, vl, wog - hp * 256 + (size_t)(shr * 4 + (i >> 1) - 1) * 8192, lds0 + SPARE + 1024);
            }
        }
    }
    int g = 8;
    auto issue_to = [&](int target) {
#pragma unroll 1
        for (; g < target; ++g) {
            asm volatile("s_waitcnt vmcnt(%0)" ::"i"(WIN - 8) : "memory");
            issue_kv(g);
        }
    };
    VM_BAR(24);
    issue_kv(8); issue_kv(9);
    VM_BAR(24);
    unsigned seen = *(const __attribute__((address_space(3))) unsigned*)(uintptr_t)(lds0 + SPARE + 512 + lane * 4);
    issue_kv(10); issue_kv(11);
    g = 12;
    PIN(seen);
    BAR_ONLY();
    issue_to(16);
#pragma unroll 1
    for (int st = 0; st < 14; ++st) {
        VM_BAR(24);
        issue_to(min(64, max(4 * st + 14, min(4 * st + 2 * RING, 2 * RING + ((TAPER * st) >> 3)))));
    }
#pragma unroll 1
    for (int i = 0; i < 8; ++i) {
        if (i == 2) VM_BAR(40);
        else if (i == 5) VM_BAR(63);
        issue_wo(i);
    }
    VM_BAR(63);
    if (m1) __hip_atomic_store(ack_out, seen, RLX);
    VM_BAR(63);
    VM_BAR(63);
    exchange_wait(syn_in, ack_in, ack_out, nonce, seen, m1, m1);
    BAR_ONLY();
    if (m3) __hip_atomic_store(ack_out, seen, RLX);
    BAR_ONLY();
    unsigned early;
    asm volatile("global_load_dword %0, %1, off sc1" : "=v"(early) : "v"(ack_in) : "memory");
    issue_wo(8);
    BAR_ONLY();
    issue_wo(9);
    asm volatile("s_waitcnt vmcnt(32)" : "+v"(early)::"memory");
    if (!__all(!m3 || early == nonce)) exchange_wait(syn_in, ack_in, ack_out, nonce, seen, m3, m1 || m3);
#pragma unroll 1
    for (int i = 10; i < 16; ++i) {
        VM_BAR(32);
        issue_wo(i);
    }
    VM_BAR(32); VM_BAR(16); VM_BAR(0);
    BAR_ONLY();
    BAR_ONLY();
    BAR_ONLY();
    if (m2) __hip_atomic_store(ack_out, seen, RLX);
    exchange_wait(syn_in, ack_in, ack_out, nonce, seen, m2, m1 || m2 || m3);
    BAR_ONLY();
    exchange_close(syn_in, ack_out, seen, m2);
}

__device__ __forceinline__ float max3(float a, float b, float c) { return __builtin_fmaxf(__builtin_fmaxf(a, b), c); }
__device__ __forceinline__ float col_max(const f32x4& a, const f32x4& b) {
    const float m = max3(max3(a[0], a[1], a[2]), max3(a[3], b[0], b[1]), max3(b[2], b[3], b[3]));
    const auto r = __builtin_amdgcn_permlane16_swap(__float_as_uint(m), __float_as_uint(m), false, false);
    const auto r0 = __builtin_amdgcn_permlane32_swap(r[0], r[1], false, false);
    const auto r1 = __builtin_amdgcn_permlane32_swap(r[1], r[0], false, false);
    return max3(max3(__uint_as_float(r0[0]), __uint_as_float(r0[1]), __uint_as_float(r1[0])), __uint_as_float(r1[1]), __uint_as_float(r1[1]));
}
__device__ __forceinline__ u32x4 exp_pack(const f32x4& a, const f32x4& b) {
    return (u32x4){pk_bf16(__builtin_amdgcn_exp2f(a[0]), __builtin_amdgcn_exp2f(a[1])), pk_bf16(__builtin_amdgcn_exp2f(a[2]), __builtin_amdgcn_exp2f(a[3])),
                   pk_bf16(__builtin_amdgcn_exp2f(b[0]), __builtin_amdgcn_exp2f(b[1])), pk_bf16(__builtin_amdgcn_exp2f(b[2]), __builtin_amdgcn_exp2f(b[3]))};
}

template <int NT>
__device__ __forceinline__ void attn_body(const char* xg, char* ogrp, char* part, char* outg, int hp, int kq, int hsel, int qt0, int xi, int rot,
                                          char* smem) {
    lds_cptr lds = (lds_cptr)smem;
    const unsigned lds0 = (unsigned)(uintptr_t)smem;
    const int lane = threadIdx.x & 63, r = lane & 15, g = lane >> 4;
    const int row_in = (r & 7) * 64 + ((g ^ (((r >> 2) & 1) << 1)) * 16);
    const int row_off = hsel * 8192 + (r >> 3) * 2048 + row_in;
    const int tr_off = hsel * 8192 + (g >> 1) * 2048 + (4 * (g & 1) + (r >> 2)) * 64 + (g & 1) * 32 + (lane & 3) * 8;

    f32x4 acc[NT][8];
#pragma unroll
    for (int t = 0; t < NT; ++t)
#pragma unroll
        for (int d = 0; d < 8; ++d) acc[t][d] = (f32x4){0.f, 0.f, 0.f, 0.f};
    bf16x8 kf[8];

    {
        const int x_off = X_RING + (2 * qt0 + (r >> 3)) * 1024 + row_in;
        const unsigned voff_x = ((lane >> 2) & 7) * (DM * 2) + (lane >> 5) * 64 + (((lane & 3) ^ (((lane >> 4) & 1) << 1)) * 16);
        auto issue_x = [&](int j) {
            const char* s = xg + (size_t)xi * 48 * (DM * 2) + ((j + rot) & 7) * 128;
            const unsigned d = lds0 + X_RING + (j & 1) * X_SLOT + xi * 6144 + 3072, v = voff_x + 3072;
            asm volatile("s_mov_b32 m0, %6\n\ts_nop 0\n\t"
                         "global_load_lds_dwordx4 %0, %7 offset:-3072\n\tglobal_load_lds_dwordx4 %1, %7 offset:-2048\n\t"
                         "global_load_lds_dwordx4 %2, %7 offset:-1024\n\tglobal_load_lds_dwordx4 %3, %7\n\t"
                         "global_load_lds_dwordx4 %4, %7 offset:1024\n\tglobal_load_lds_dwordx4 %5, %7 offset:2048"
                         ::"v"(v), "v"(v + 7168), "v"(v + 2 * 7168), "v"(v + 3 * 7168), "v"(v + 4 * 7168), "v"(v + 5 * 7168), "s"(d), "s"(s) : "memory");
        };
        if (NT == 2) issue_x(1);
#pragma unroll
        for (int t = 0; t < NT; ++t)
#pragma unroll
            for (int d = 0; d < 8; ++d) PIN(acc[t][d]);
        lds_cptr x0p = lds_at(lds0 + x_off), w0p = lds_at(lds0 + W_RING + 2 * W_SLOT + tr_off), w1p = lds_at(lds0 + W_RING + 2 * W_SLOT + (tr_off ^ 32));
        if (NT == 2) VM_BAR(6); else VM_BAR(0);
        bf16x8 wa[8], wb[8], xa[NT], xb[NT];
#pragma unroll
        for (int t = 0; t < NT; ++t) xa[t] = lds_row_frag(x0p + t * 2048);
#pragma unroll
        for (int d = 0; d < 8; ++d) wa[d] = lds_tr_frag(((d & 1) ? w1p : w0p) + (d >> 1) * 512);
        int s_nxt = 0;
#define QREAD(n, WN, XN) do { \
            if ((n) < 16) { hf[(n) & 1] = lds_tr_half((((n) & 2) ? wp1 : wp0) + ((n) >> 2) * 512 + ((n) & 1) * 4096); if ((n) & 1) WN[(n) >> 1] = join(hf[0], hf[1]); } \
            else if ((n) < 16 + NT) XN[(n) - 16] = lds_row_frag(xp + ((n) - 16) * 2048); } while (0)
#define QSTEP(WC, XC, WN, XN, XNO, SYNC) do { \
            lds_cptr wp0 = lds_at(lds0 + W_RING + s_nxt + tr_off), wp1 = lds_at(lds0 + W_RING + s_nxt + (tr_off ^ 32)); \
            lds_cptr xp = lds_at(lds0 + x_off + (XNO)); \
            s16x4 hf[2]; \
            LGKM0(); SYNC; SB(); \
            _Pragma("unroll") for (int n = 0; n < 8 * NT; ++n) { \
                acc[n % NT][n / NT] = MFMA(WC[n / NT], XC[n % NT], acc[n % NT][n / NT]); \
                SB(); \
                QREAD(n, WN, XN); SB(); \
            } \
            if (NT == 2) { XN[0] = lds_row_frag(xp); XN[1] = lds_row_frag(xp + 2048); SB(); } \
            s_nxt = s_nxt == 2 * W_SLOT ? 0 : s_nxt + W_SLOT; } while (0)
#pragma unroll 1
        for (int it = 0; it < 7; ++it) {
            QSTEP(wa, xa, wb, xb, (it & 1) * X_SLOT + 512, BAR_ONLY());
            QSTEP(wb, xb, wa, xa, ((it + 1) & 1) * X_SLOT, if (NT == 2) VM_BAR(0); else BAR_ONLY());
            if (NT == 2 && it < 6) issue_x(it + 2);
        }
        QSTEP(wa, xa, wb, xb, X_SLOT + 512, BAR_ONLY());
        lds_cptr k0p = lds_at(lds0 + row_off);
        LGKM0(); BAR_ONLY(); SB();
#pragma unroll
        for (int d = 0; d < 8; ++d) {
#pragma unroll
            for (int t = 0; t < NT; ++t) acc[t][d] = MFMA(wb[d], xb[t], acc[t][d]);
            kf[d] = lds_row_frag(k0p + (d >> 2) * 4096 + (d & 3) * 512);
            SB();
        }
#undef QSTEP
#undef QREAD
    }
    bf16x8 qf[NT][4];
#pragma unroll
    for (int t = 0; t < NT; ++t)
#pragma unroll
        for (int s = 0; s < 4; ++s) {
            const f32x4 u = acc[t][2 * s] * QSCALE, v = acc[t][2 * s + 1] * QSCALE;
            qf[t][s] = __builtin_bit_cast(bf16x8, regroup(pk_bf16(u[0], u[1]), pk_bf16(u[2], u[3]), pk_bf16(v[0], v[1]), pk_bf16(v[2], v[3])));
        }

    f32x4 negm[NT], lacc[NT], sc[NT][2];
    SB();
#pragma unroll
    for (int n = 0; n < 8 * NT; ++n) {
        const int j = n / NT, t = n % NT;
        sc[t][j >> 2] = MFMA(kf[j], qf[t][j & 3], (j & 3) == 0 ? ((f32x4){0.f, 0.f, 0.f, 0.f}) : sc[t][j >> 2]);
        SB();
        acc[t][j] = (f32x4){0.f, 0.f, 0.f, 0.f};
        PIN(acc[t][j]);
        SB();
    }
#pragma unroll
    for (int t = 0; t < NT; ++t) {
        const float nm = -(col_max(sc[t][0], sc[t][1]) + PBIAS);
        negm[t] = (f32x4){nm, nm, nm, nm};
        sc[t][0] += nm; sc[t][1] += nm;
        lacc[t] = (f32x4){0.f, 0.f, 0.f, 0.f};
    }
    u32x4 ones = {0x3f803f80u, 0x3f803f80u, 0x3f803f80u, 0x3f803f80u};
    PIN(ones);

#define VHALF(n) do { hf[(n) & 1] = lds_tr_half((((n) & 2) ? vp1 : vp0) + ((n) >> 2) * 512 + ((n) & 1) * 4096); if ((n) & 1) vf[(n) >> 1] = join(hf[0], hf[1]); } while (0)
#define KREAD(j) kf[j] = lds_row_frag(kp + ((j) >> 2) * 4096 + ((j) & 3) * 512)
#define PFRAG(t) __builtin_bit_cast(bf16x8, pf[t])
#define ONES __builtin_bit_cast(bf16x8, ones)
#define RESCALE_IF_NEEDED(kc) do { \
        unsigned any = 0; \
        _Pragma("unroll") for (int t = 0; t < NT; ++t) { any |= pf[t][0] | pf[t][1]; any |= pf[t][2] | pf[t][3]; } \
        if (__builtin_expect(__any((any & 0x40004000u) != 0), 0)) { \
            _Pragma("unroll") for (int t = 0; t < NT; ++t) { \
                f32x4 s0 = negm[t], s1 = negm[t]; \
                _Pragma("unroll") for (int j = 0; j < 4; ++j) { \
                    s0 = MFMA(lds_row_frag((kc) + j * 512), qf[t][j], s0); SB(); \
                    s1 = MFMA(lds_row_frag((kc) + 4096 + j * 512), qf[t][j], s1); SB(); } \
                const float dl = fmaxf(col_max(s0, s1) + PBIAS, 0.f); \
                const float alpha = __builtin_amdgcn_exp2f(-dl); \
                negm[t] -= dl; \
                lacc[t] *= alpha; \
                _Pragma("unroll") for (int d = 0; d < 8; ++d) acc[t][d] *= alpha; \
                pf[t] = exp_pack(s0 - dl, s1 - dl); \
                SB(); \
            } \
        } \
        SB(); } while (0)
    bf16x8 vf[8];
    s16x4 hf[2];
    u32x4 pf[NT];
    if (NT == 3) {
        int p_k = 0;
#pragma unroll 1
        for (int st = 0; st < 16; ++st) {
            const int p_c = p_k, p_v = p_k + HALF;
            p_k = ring_next(p_k, 2);
            lds_cptr vp0 = lds_at(lds0 + p_v + tr_off), vp1 = lds_at(lds0 + p_v + (tr_off ^ 32));
            lds_cptr kp = lds + p_k + row_off;
            __builtin_amdgcn_s_setprio(2);
            LGKM0(); BAR_ONLY();
            SB();
#pragma unroll
            for (int n = 0; n < 24; ++n) {
                const int j = n / 3, t = n % 3;
                sc[t][j >> 2] = MFMA(kf[j], qf[t][j & 3], (j & 3) == 0 ? negm[t] : sc[t][j >> 2]);
                SB();
                if (t != 1 && n >= 2) VHALF(t == 2 ? 2 * j : 2 * j - 1);
                if (n >= 14 && n - 14 < EARLY) { float e_ = __builtin_amdgcn_exp2f(sc[(n - 14) >> 2][0][(n - 14) & 3]); PIN(e_); sc[(n - 14) >> 2][0][(n - 14) & 3] = e_; }
                SB();
            }
            VHALF(15);
#pragma unroll
            for (int i = EARLY; i < 12; ++i) sc[i >> 2][0][i & 3] = __builtin_amdgcn_exp2f(sc[i >> 2][0][i & 3]);
#pragma unroll
            for (int t = 0; t < NT; ++t) {
                pf[t][0] = pk_bf16(sc[t][0][0], sc[t][0][1]); pf[t][1] = pk_bf16(sc[t][0][2], sc[t][0][3]);
                pf[t][2] = pk_bf16(__builtin_amdgcn_exp2f(sc[t][1][0]), __builtin_amdgcn_exp2f(sc[t][1][1]));
                pf[t][3] = pk_bf16(__builtin_amdgcn_exp2f(sc[t][1][2]), __builtin_amdgcn_exp2f(sc[t][1][3]));
            }
            RESCALE_IF_NEEDED(lds + p_c + row_off);
            __builtin_amdgcn_s_setprio(0);
#pragma unroll
            for (int n = 0; n < 24; ++n) {
                acc[n % 3][n / 3] = MFMA(vf[n / 3], PFRAG(n % 3), acc[n % 3][n / 3]);
                SB();
                if (n < 16 && (n & 1)) { KREAD(n >> 1); SB(); }
            }
#pragma unroll
            for (int t = 0; t < NT; ++t) lacc[t] = MFMA(ONES, PFRAG(t), lacc[t]);
            SB();
        }
    } else {
        {
            lds_cptr vp0 = lds + HALF + tr_off, vp1 = lds + HALF + (tr_off ^ 32);
#pragma unroll
            for (int n = 0; n < 16; ++n) VHALF(n);
#pragma unroll
            for (int t = 0; t < NT; ++t) pf[t] = exp_pack(sc[t][0], sc[t][1]);
        }
        int p_k = 0;
#pragma unroll 1
        for (int st = 0; st < 15; ++st) {
            p_k = ring_next(p_k, 2);
            lds_cptr vp0 = lds_at(lds0 + p_k + HALF + tr_off), vp1 = lds_at(lds0 + p_k + HALF + (tr_off ^ 32));
            lds_cptr kp = lds_at(lds0 + p_k + row_off);
            LGKM0(); BAR_ONLY();
            SB();
#pragma unroll
            for (int n = 0; n < 16; ++n) {
                acc[n & 1][n >> 1] = MFMA(vf[n >> 1], PFRAG(n & 1), acc[n & 1][n >> 1]);
                SB();
                if (n < 8) { KREAD(n); SB(); }
            }
#pragma unroll
            for (int t = 0; t < NT; ++t) lacc[t] = MFMA(ONES, PFRAG(t), lacc[t]);
            SB();
#pragma unroll
            for (int n = 0; n < 16; ++n) {
                const int j = n >> 1, t = n & 1;
                sc[t][j >> 2] = MFMA(kf[j], qf[t][j & 3], (j & 3) == 0 ? negm[t] : sc[t][j >> 2]);
                SB();
                VHALF(n);
                SB();
            }
#pragma unroll
            for (int t = 0; t < NT; ++t) pf[t] = exp_pack(sc[t][0], sc[t][1]);
            RESCALE_IF_NEEDED(kp);
        }
        BAR_ONLY();
#pragma unroll
        for (int n = 0; n < 16; ++n) acc[n & 1][n >> 1] = MFMA(vf[n >> 1], PFRAG(n & 1), acc[n & 1][n >> 1]);
#pragma unroll
        for (int t = 0; t < NT; ++t) lacc[t] = MFMA(ONES, PFRAG(t), lacc[t]);
    }
#undef RESCALE_IF_NEEDED
#undef ONES
#undef PFRAG
#undef KREAD
#undef VHALF
    unsigned lane2;
    asm volatile("v_mbcnt_lo_u32_b32 %0, -1, 0\n\tv_mbcnt_hi_u32_b32 %0, -1, %0" : "=v"(lane2));
    const auto orsrc = __builtin_amdgcn_make_buffer_rsrc(ogrp + hp * 2 * HEAD_O, 0, 2 * HEAD_O, 0x00020000);
    const unsigned o_off = hsel * HEAD_O + qt0 * 1024 + lane2 * 16;
    unsigned o_st[4];
#pragma unroll
    for (int s = 0; s < 4; ++s) { o_st[s] = o_off + s * NQT * 1024; PIN(o_st[s]); }
    u32x4 of[4][NT];
    float inv[NT];
#pragma unroll
    for (int t = 0; t < NT; ++t) inv[t] = __builtin_amdgcn_rcpf(lacc[t][0]);
#define OSTORE(s) do { _Pragma("unroll") for (int t = 0; t < NT; ++t) \
        __builtin_amdgcn_raw_buffer_store_b128(of[s][t], orsrc, o_st[s] + t * 1024, 0, 16); SB(); } while (0)
    bf16x8 wa[8], wb[8];
#pragma unroll
    for (int s = 0; s < 4; ++s) {
#pragma unroll
        for (int t = 0; t < NT; ++t) {
            const f32x4 u = acc[t][2 * s] * inv[t], v = acc[t][2 * s + 1] * inv[t];
            of[s][t] = (u32x4){pk_bf16(u[0], u[1]), pk_bf16(u[2], u[3]), pk_bf16(v[0], v[1]), pk_bf16(v[2], v[3])};
            PIN(of[s][t]);
        }
        if (s < 2) OSTORE(s);
        if (s == 1) {
#pragma unroll
            for (int d = 0; d < 8; ++d) wa[d] = lds_tr_frag(lds + (32 % RING) * HALF + ((d & 1) ? (tr_off ^ 32) : tr_off) + (d >> 1) * 512);
            SB();
        }
    }
    __builtin_amdgcn_s_setprio(NT == 2 ? 1 : 0);
#define OLOAD(s, step) do { const int j_ = (step) - 4, q_ = (j_ * 11) >> 5; \
        const char* b_ = ogrp + ((hp ^ (1 + j_ - 3 * q_)) & 3) * 2 * HEAD_O + (q_ & 3) * NQT * 1024; \
        if (NT == 3) asm volatile("s_cmp_ge_u32 %5, 12\n\ts_cbranch_scc1 1f\n\tglobal_load_dwordx4 %0, %3, %4 sc1\n\tglobal_load_dwordx4 %1, %3, %4 offset:1024 sc1\n\t" \
                                  "global_load_dwordx4 %2, %3, %4 offset:2048 sc1\n1:" \
                                  : "+v"(of[s][0]), "+v"(of[s][1]), "+v"(of[s][NT - 1]) : "v"(o_off), "s"(b_), "s"(j_) : "memory", "scc"); \
        else asm volatile("s_cmp_ge_u32 %4, 12\n\ts_cbranch_scc1 1f\n\tglobal_load_dwordx4 %0, %2, %3 sc1\n\tglobal_load_dwordx4 %1, %2, %3 offset:1024 sc1\n1:" \
                          : "+v"(of[s][0]), "+v"(of[s][1]) : "v"(o_off), "s"(b_), "s"(j_) : "memory", "scc"); } while (0)
#define OWAIT(s) do { \
        if (NT == 3) asm volatile("s_waitcnt vmcnt(%4)\n\ts_cmp_lg_u32 %3, 3\n\ts_cbranch_scc1 1f\n\ts_waitcnt vmcnt(%5)\n1:" \
                                  : "+v"(of[s][0]), "+v"(of[s][1]), "+v"(of[s][NT - 1]) : "s"(ph), "i"(3 * NT), "i"((3 - (s)) * NT) : "memory", "scc"); \
        else asm volatile("s_waitcnt vmcnt(%3)\n\ts_cmp_lg_u32 %2, 3\n\ts_cbranch_scc1 1f\n\ts_waitcnt vmcnt(%4)\n1:" \
                          : "+v"(of[s][0]), "+v"(of[s][1]) : "s"(ph), "i"(3 * NT), "i"((3 - (s)) * NT) : "memory", "scc"); } while (0)
#define OSTEP(step, s, WC, WN, FIRST, SYNC, TAIL) do { \
        en = ring_next(en, 1); \
        lds_cptr wp0 = lds_at(lds0 + en + tr_off), wp1 = lds_at(lds0 + en + (tr_off ^ 32)); \
        LGKM0(); \
        SYNC; \
        SB(); \
        _Pragma("unroll") for (int n = 0; n < 8 * NT; ++n) { \
            acc[n % NT][n / NT] = MFMA(WC[n / NT], __builtin_bit_cast(bf16x8, of[s][n % NT]), FIRST ? ((f32x4){0.f, 0.f, 0.f, 0.f}) : acc[n % NT][n / NT]); \
            SB(); \
            if (n < 16) { hf[n & 1] = lds_tr_half(((n & 2) ? wp1 : wp0) + (n >> 2) * 512 + (n & 1) * 4096); if (n & 1) WN[n >> 1] = join(hf[0], hf[1]); SB(); } \
        } \
        TAIL; \
        SB(); } while (0)
    int en = (32 % RING) * HALF;
    OSTEP(0, 0, wa, wb, true, VM_BAR(0); if (NT == 3) { OSTORE(2); OSTORE(3); }, if (NT == 2) { OSTORE(2); OSTORE(3); });
    OSTEP(1, 1, wb, wa, false, BAR_ONLY(), );
    OSTEP(2, 2, wa, wb, false, BAR_ONLY(), );
    OSTEP(3, 3, wb, wa, false, VM_BAR(0); OLOAD(0, 4); OLOAD(1, 5); OLOAD(2, 6), if (NT == 2) OLOAD(3, 7));
#define OSTEP_P(s, WC, WN) OSTEP(4 * ph + (s), s, WC, WN, false, \
        if (NT == 2) OWAIT(s); BAR_ONLY(); if (NT == 3) { OLOAD(((s) + 3) & 3, 4 * ph + (s) + 3); OWAIT(s); }, if (NT == 2) OLOAD(s, 4 * ph + (s) + 4))
#pragma unroll 1
    for (int ph = 1; ph < 4; ++ph) {
        OSTEP_P(0, wa, wb); OSTEP_P(1, wb, wa); OSTEP_P(2, wa, wb); OSTEP_P(3, wb, wa);
    }
#undef OSTEP_P
#undef OSTORE
    LGKM0();
#undef OWAIT
#undef OSTEP
#undef OLOAD
    const int yq = (2 * hsel) ^ (kq & 1);
    {
        char* red = smem + qt0 * 4096 + lane2 * 16;
#pragma unroll
        for (int t = 0; t < NT; ++t)
#pragma unroll
            for (int c = 0; c < 4; ++c) *(f32x4*)(red + hsel * (NQT * 4096) + t * 4096 + c * 1024) = acc[t][4 + c];
        LGKM0(); BAR_ONLY();
#pragma unroll
        for (int t = 0; t < NT; ++t)
#pragma unroll
            for (int c = 0; c < 4; ++c) acc[t][c] += *(const f32x4*)(red + (hsel ^ 1) * (NQT * 4096) + t * 4096 + c * 1024);
    }
    const auto prsrc = __builtin_amdgcn_make_buffer_rsrc(part, 0, 16 * PART_Q, 0x00020000);
    const unsigned p_off = qt0 * 1024 + lane2 * 16;
#pragma unroll
    for (int u = 1; u >= 0; --u)
        if (u == 1 || yq != kq) {
            const auto dst = __builtin_amdgcn_make_buffer_rsrc(part + ((yq ^ u) * 4 + kq) * PART_Q, 0, PART_Q, 0x00020000);
#pragma unroll
            for (int t = 0; t < NT; ++t) {
                const f32x4 a = acc[t][2 * u], c = acc[t][2 * u + 1];
                const u32x4 pk = {pk_bf16(a[0], a[1]), pk_bf16(a[2], a[3]), pk_bf16(c[0], c[1]), pk_bf16(c[2], c[3])};
                __builtin_amdgcn_raw_buffer_store_b128(pk, dst, p_off + t * 1024, 0, 16);
            }
        }
    VM_BAR(0);
    BAR_ONLY();
    if (yq == kq) {
        u32x4 in[3][NT];
#pragma unroll
        for (int j = 0; j < 3; ++j)
#pragma unroll
            for (int t = 0; t < NT; ++t) in[j][t] = __builtin_amdgcn_raw_buffer_load_b128(prsrc, p_off + t * 1024, (kq * 4 + (kq ^ (j + 1))) * PART_Q, 16);
        const auto outr = __builtin_amdgcn_make_buffer_rsrc(outg + kq * 64, 0, (SQ - 1) * (2 * DM * 2) + 64, 0x00020000);
#pragma unroll
        for (int t = 0; t < NT; ++t) {
            f32x4 u = acc[t][0], v = acc[t][1];
#pragma unroll
            for (int j = 0; j < 3; ++j) {
                const u32x4 x = in[j][t];
                u += (f32x4){__uint_as_float(x[0] << 16), __uint_as_float(x[0] & 0xffff0000u), __uint_as_float(x[1] << 16), __uint_as_float(x[1] & 0xffff0000u)};
                v += (f32x4){__uint_as_float(x[2] << 16), __uint_as_float(x[2] & 0xffff0000u), __uint_as_float(x[3] << 16), __uint_as_float(x[3] & 0xffff0000u)};
            }
            const u32x4 o = regroup(pk_bf16(u[0], u[1]), pk_bf16(u[2], u[3]), pk_bf16(v[0], v[1]), pk_bf16(v[2], v[3]));
            __builtin_amdgcn_raw_buffer_store_b128(o, outr, ((qt0 + t) * 16 + (lane2 & 15)) * (2 * DM * 2) + (lane2 >> 4) * 16, 0, 0);
        }
    }
}

__global__ __launch_bounds__(512, 2) void attn_kernel(const char* xA, const char* kA, const char* vA, const char* wqA, const char* woA,
                                                      const char* xB, const char* kB, const char* vB, const char* wqB, const char* woB,
                                                      char* ws, char* out) {
    extern __shared__ __attribute__((aligned(16))) char smem[];
    unsigned long long pc;
    asm volatile("s_getpc_b64 %0" : "=s"(pc));
    const int bid = blockIdx.x, blk = (bid >> 2) & 1, kq = bid & 3, hp = (bid >> 3) & 3, b = bid >> 5;
    const int h0 = kq * 8 + hp * 2;
    const int w = __builtin_amdgcn_readfirstlane(threadIdx.x >> 6);
    const int rot = (b + 2 * hp) & 4;
    const char* xg = (blk ? xB : xA) + (size_t)b * SQ * DM * 2;
    const char* wg = (blk ? wqB : wqA) + (size_t)h0 * DH * 2;
    {
        const int lane = threadIdx.x & 63, e = (lane >> 2) & 7, colb = (lane >> 5) * 64 + (((lane & 3) ^ (((lane >> 4) & 1) << 1)) * 16);
        const unsigned lds0 = (unsigned)(uintptr_t)smem;
        const char* sw = wg + (size_t)(2 * rot * 32 + 16 * ((w >> 1) & 1) + 4 * (w >> 2)) * (NH * DH * 2) + (w & 1) * 256;
        const unsigned dw = lds0 + W_RING + 2 * W_SLOT + (w >> 1) * 2048 + (w & 1) * 8192;
        const unsigned vw = (8 * (e >> 2) + (e & 3)) * (NH * DH * 2) + colb, vx = e * (DM * 2) + colb;
        GLDS(0, vw, sw, dw); GLDS(128, vw, sw, dw + 1024);
        const char* sx = xg + (size_t)w * 8 * (DM * 2) + rot * 128;
        GLDS(0, vx, sx, lds0 + X_RING + w * 1024);
        GLDS(0, vx, sx + 64 * (DM * 2), lds0 + X_RING + w * 1024 + 8192);
        if (w < 2) GLDS(0, vx, sx + 128 * (DM * 2), lds0 + X_RING + w * 1024 + 16384);
    }
    char* ogrp = ws + WS_O + (size_t)((blk * NB + b) * NH + kq * 8) * HEAD_O;
    char* part = ws + WS_PART + (size_t)((blk * NB + b) * 4 + hp) * 16 * PART_Q;
    char* outg = out + (size_t)b * SQ * (2 * DM * 2) + blk * (DM * 2) + hp * 256;
    if (w == 0) {
        const char* kg = (blk ? kB : kA) + (size_t)(b * NH + h0) * SK * (DH * 2);
        const char* vg = (blk ? vB : vA) + (size_t)(b * NH + h0) * SK * (DH * 2);
        const char* wog = (blk ? woB : woA) + (size_t)kq * 1024 * (DM * 2) + hp * 256;
        const char* code = (const char*)(pc & ~255ull) + min((hp * 2 + (b & 1)) * 8192 + (int)threadIdx.x * 128, CODE_BYTES - 128);
        __builtin_amdgcn_s_setprio(3);
        attn_loader(wg, kg, vg, wog, ws, code, hp, bid >> 3, rot, (unsigned)(uintptr_t)smem);
    } else if (w <= 4) attn_body<3>(xg, ogrp, part, outg, hp, kq, w >> 2, w == 4 ? 0 : 3 * (w - 1), 0, rot, smem);
    else {
        __builtin_amdgcn_s_setprio(1);
        attn_body<2>(xg, ogrp, part, outg, hp, kq, 1, 2 * w - 7, w - 5, rot, smem);
    }
}

extern "C" void kernel_launch(void* const* d_in, const int* in_sizes, int n_in, void* d_out, int out_size, void* d_ws, size_t ws_size,
                              hipStream_t stream) {
    (void)in_sizes; (void)n_in; (void)out_size; (void)ws_size;
    (void)hipFuncSetAttribute((const void*)attn_kernel, hipFuncAttributeMaxDynamicSharedMemorySize, ATTN_LDS);
    attn_kernel<<<256, 512, ATTN_LDS, stream>>>((const char*)d_in[0], (const char*)d_in[1], (const char*)d_in[2], (const char*)d_in[3],
                                                (const char*)d_in[4], (const char*)d_in[5], (const char*)d_in[6], (const char*)d_in[7],
                                                (const char*)d_in[8], (const char*)d_in[9], (char*)d_ws, (char*)d_out);
}
```
